```python
import torch

def _softmax(x, axis=-1):
    return torch.softmax(x, dim=axis)
def _gelu(x):
    return torch.nn.functional.gelu(x, approximate="tanh")
def _silu(x):
    return torch.nn.functional.silu(x)
def _sigmoid(x):
    return torch.sigmoid(x)
def _layernorm(x, axis=-1, eps=1e-5):
    m = x.mean(dim=axis, keepdim=True)
    v = x.var(dim=axis, keepdim=True, unbiased=False)
    return (x - m) / torch.sqrt(v + eps)
def _scatter_add(tgt, idx, src, axis=0):
    return tgt.clone().index_add_(axis, idx, src)
def _segment_sum(src, seg, n):
    out = torch.zeros((n,) + src.shape[1:], dtype=src.dtype)
    return out.index_add_(0, seg, src)
def _pad(x, pw):
    flat = tuple(v for pair in reversed(pw) for v in pair)
    return torch.nn.functional.pad(x, flat)

kernel_name = 'parallel_sum__sliding_block__dense_activation_squared_relu__prompt'

def setup_inputs():

    _w = (lambda a: (lambda t: t.to(torch.bfloat16) if t.is_floating_point() else t)(torch.as_tensor(a)))
    def _A_setup():
        B, S, D = 1, 2048, 448
        return {
            "x": _w((lambda *s: torch.randn(*s, dtype=torch.bfloat16) * 0.05)(B, S, D)),
            "wqkv": _w((lambda *s: torch.randn(*s, dtype=torch.bfloat16) * 0.1)(D, 3 * D)),
            "wo": _w((lambda *s: torch.randn(*s, dtype=torch.bfloat16) * 0.05)(D, D)),
        }
    _A_in = _A_setup()

    def _B_setup():
        B, S, D, F = 1, 2048, 448, 1792
        return {
            "x": _w((lambda *s: torch.randn(*s, dtype=torch.bfloat16) * 0.05)(B, S, D)),
            "w1": _w((lambda *s: torch.randn(*s, dtype=torch.bfloat16) * 0.05)(D, F)),
            "b1": _w((lambda *s: torch.randn(*s, dtype=torch.bfloat16) * 0.4)(F)),
            "w2": _w((lambda *s: torch.randn(*s, dtype=torch.bfloat16) * 0.05)(F, D)),
            "b2": _w((lambda *s: torch.randn(*s, dtype=torch.bfloat16) * 1.6)(D)),
        }
    _B_in = _B_setup()

    return {
        "A_x": _A_in["x"],
        "A_wqkv": _A_in["wqkv"],
        "A_wo": _A_in["wo"],
        "B_x": _B_in["x"],
        "B_w1": _B_in["w1"],
        "B_b1": _B_in["b1"],
        "B_w2": _B_in["w2"],
        "B_b2": _B_in["b2"],
    }

def reference(A_x, A_wqkv, A_wo, B_x, B_w1, B_b1, B_w2, B_b2):
    def _A_ref(x, wqkv, wo):
        B, S, D = x.shape
        H = 16; Dh = D // H; W = 256
        qkv = (lambda x, *d: x.permute(*d))((x @ wqkv).reshape(B, S, 3, H, Dh), 2, 0, 3, 1, 4)
        q, k, v = qkv[0], qkv[1], qkv[2]
        scores = torch.einsum("bhqd,bhkd->bhqk", q, k) * (Dh ** -0.5)
        idx = torch.arange(S)
        mask = (idx[:, None] - idx[None, :] >= 0) & (idx[:, None] - idx[None, :] < W)
        scores = torch.where(mask[None, None], scores, -1e9)
        p = _softmax(scores, axis=-1)
        o = torch.einsum("bhqk,bhkd->bhqd", p, v)
        return (lambda x, *d: x.permute(*d))(o, 0, 2, 1, 3).reshape(B, S, D) @ wo

    def _B_ref(x, w1, b1, w2, b2):
        h = torch.einsum("bsd,df->bsf", x, w1) + b1
        zero = torch.zeros_like(h)
        r = torch.where(h > zero, h, zero)
        h = r * r
        out = torch.einsum("bsf,fd->bsd", h, w2) + b2
        return out

    x = A_x
    xn = _layernorm(x, axis=-1)
    return x + _A_ref(xn, A_wqkv, A_wo) + _B_ref(xn, B_w1, B_b1, B_w2, B_b2)
```

```cpp
#include <hip/hip_runtime.h>
#include <hip/hip_bf16.h>
#include <stdint.h>

typedef unsigned short u16;
static __device__ __forceinline__ float bf2f(u16 v) { return __uint_as_float(((unsigned)v) << 16); }
static __device__ __forceinline__ u16 f2bf(float f) { unsigned u = __float_as_uint(f); u += 0x7fffu + ((u >> 16) & 1u); return (u16)(u >> 16); }
static __device__ __forceinline__ float rbf(float f) { return bf2f(f2bf(f)); }

constexpr int S = 2048, D = 448, F = 1792, H = 16, DH = 28, W = 256;

__global__ void ln_k(const u16* x, u16* xn) {
    int row = blockIdx.x; int lane = threadIdx.x;
    float v[7]; float s = 0;
    for (int i = 0; i < 7; i++) { v[i] = bf2f(x[row * D + lane + 64 * i]); s += v[i]; }
    for (int m = 32; m >= 1; m >>= 1) s += __shfl_xor(s, m);
    float mean = s / D; float q = 0;
    for (int i = 0; i < 7; i++) { float d = v[i] - mean; q += d * d; }
    for (int m = 32; m >= 1; m >>= 1) q += __shfl_xor(q, m);
    float var = rbf(q / D); float mb = rbf(mean);
    float sd = rbf(sqrtf(rbf(var + 1e-5f)));
    for (int i = 0; i < 7; i++) xn[row * D + lane + 64 * i] = f2bf(rbf(v[i] - mb) / sd);
}
__global__ void gemm_k(const u16* A, const u16* B, const u16* bias, u16* C, int N, int K, int mode) {
    int col = blockIdx.x * 64 + threadIdx.x; int row = blockIdx.y;
    if (col >= N) return;
    float acc = 0;
    for (int k = 0; k < K; k++) acc += bf2f(A[row * K + k]) * bf2f(B[k * N + col]);
    acc = rbf(acc);
    if (mode == 1) { acc = rbf(acc + bf2f(bias[col])); acc = acc > 0 ? acc : 0; acc = acc * acc; }
    if (mode == 2) { acc = rbf(acc + bf2f(bias[col])); }
    C[row * N + col] = f2bf(acc);
}
__global__ void attn_k(const u16* qkv, u16* o) {
    int row = blockIdx.x, h = blockIdx.y; int t = threadIdx.x;
    __shared__ float p[256]; __shared__ float red[4];
    int key = row - 255 + t; float s = -1e30f;
    if (key >= 0) { float a = 0; for (int d = 0; d < DH; d++) a += bf2f(qkv[row * 1344 + h * DH + d]) * bf2f(qkv[key * 1344 + 448 + h * DH + d]); s = rbf(rbf(a) * 0.1889822365f); }
    float m = s; for (int k = 32; k >= 1; k >>= 1) m = fmaxf(m, __shfl_xor(m, k));
    if ((t & 63) == 0) red[t >> 6] = m; __syncthreads();
    m = fmaxf(fmaxf(red[0], red[1]), fmaxf(red[2], red[3])); __syncthreads();
    float e = key >= 0 ? __expf(s - m) : 0.f; float l = e; for (int k = 32; k >= 1; k >>= 1) l += __shfl_xor(l, k);
    if ((t & 63) == 0) red[t >> 6] = l; __syncthreads();
    l = red[0] + red[1] + red[2] + red[3];
    p[t] = rbf(e / l); __syncthreads();
    if (t < DH) { float a = 0; for (int j = 0; j < 256; j++) { int kk = row - 255 + j; if (kk >= 0) a += p[j] * bf2f(qkv[kk * 1344 + 896 + h * DH + t]); } o[row * D + h * DH + t] = f2bf(a); }
}
__global__ void fin_k(const u16* x, const u16* a, const u16* b, u16* out) {
    int i = blockIdx.x * 256 + threadIdx.x;
    out[i] = f2bf(rbf(bf2f(x[i]) + bf2f(a[i])) + bf2f(b[i]));
}

extern "C" void kernel_launch(void* const* d_in, const int* in_sizes, int n_in, void* d_out, int out_size, void* d_ws, size_t ws_size, hipStream_t stream) {
    const u16* x = (const u16*)d_in[0]; const u16* wqkv = (const u16*)d_in[1]; const u16* wo = (const u16*)d_in[2];
    const u16* w1 = (const u16*)d_in[4]; const u16* b1 = (const u16*)d_in[5]; const u16* w2 = (const u16*)d_in[6]; const u16* b2 = (const u16*)d_in[7];
    u16* ws = (u16*)d_ws;
    u16* xn = ws; u16* qkv = xn + S * D; u16* h = qkv + S * 1344; u16* o = h + S * F; u16* a = o + S * D; u16* b = a + S * D;
    ln_k<<<S, 64, 0, stream>>>(x, xn);
    gemm_k<<<dim3(21, S), 64, 0, stream>>>(xn, wqkv, nullptr, qkv, 1344, D, 0);
    gemm_k<<<dim3(28, S), 64, 0, stream>>>(xn, w1, b1, h, F, D, 1);
    attn_k<<<dim3(S, H), 256, 0, stream>>>(qkv, o);
    gemm_k<<<dim3(7, S), 64, 0, stream>>>(o, wo, nullptr, a, D, D, 0);
    gemm_k<<<dim3(7, S), 64, 0, stream>>>(h, w2, b2, b, D, F, 2);
    fin_k<<<S * D / 256, 256, 0, stream>>>(x, a, b, (u16*)d_out);
}
```

```cpp
#include <hip/hip_runtime.h>
#include <stdint.h>

typedef unsigned short u16;
typedef unsigned long long u64;
typedef short bf16x8 __attribute__((ext_vector_type(8)));
typedef short s16x4 __attribute__((ext_vector_type(4)));
typedef __bf16 b16x2 __attribute__((ext_vector_type(2)));
typedef float f32x4 __attribute__((ext_vector_type(4)));
typedef float f32x2 __attribute__((ext_vector_type(2)));
typedef unsigned u32x4 __attribute__((ext_vector_type(4)));
typedef unsigned u32x2 __attribute__((ext_vector_type(2)));
#define LAS __attribute__((address_space(3)))
#define GAS __attribute__((address_space(1)))
#define DI __device__ __forceinline__
#define RLX_AGENT __ATOMIC_RELAXED, __HIP_MEMORY_SCOPE_AGENT

constexpr unsigned WS_E = 0;
constexpr unsigned WS_FQ = 4096;
constexpr unsigned WS_FH = 32768;
constexpr unsigned WS_FO = 65536;
constexpr unsigned WS_DBG = 98304;
constexpr unsigned WS_Q = 262144;
constexpr unsigned WS_K = WS_Q + 16 * 64 * 1792;
constexpr unsigned WS_V = WS_K + 16 * 64 * 1792;
constexpr unsigned WS_H = WS_V + 16 * 64 * 2048;
constexpr unsigned WS_O = WS_H + 128 * 56 * 1024;
constexpr unsigned WS_END = WS_O + 128 * 16 * 1024;

constexpr int UNIT = 8192;
__host__ __device__ constexpr int phys(int piece) { return (((piece % 19) + 3) % 19) * UNIT; }
constexpr int L_REG = 12 * UNIT;
constexpr int L_Q = L_REG, L_K = L_REG + 7168, L_V = L_REG + 28672;
constexpr int L_TB = 19 * UNIT;
constexpr int L_RS = L_TB, L_CS = L_TB + 2048, L_ST = L_TB + 4096, L_B1 = L_TB + 6144, L_B2 = L_TB + 6528;
constexpr int L_RED = 4 * UNIT, RED_RS = 272;
constexpr int LDS_BYTES = 163840;

#define NEARLY 12
#define OFLAG_AT 3
constexpr unsigned SPIN_MAX = 400000;

#define MFMA(a, b, c) __builtin_amdgcn_mfma_f32_16x16x32_bf16((a), (b), (c), 0, 0, 0)
#define WAITBAR(N) asm volatile("s_waitcnt vmcnt(%0) lgkmcnt(0)\n\ts_barrier" ::"i"(N) : "memory")
#define BAR() asm volatile("s_waitcnt lgkmcnt(0)\n\ts_barrier" ::: "memory")
#define WAITV(N) asm volatile("s_waitcnt vmcnt(%0)" ::"i"(N) : "memory")

DI void dma16(unsigned voff, const char* sbase, unsigned ldsdst) {
    asm volatile("s_mov_b32 m0, %2\n\ts_nop 0\n\tglobal_load_lds_dwordx4 %0, %1" ::"v"(voff), "s"(sbase), "s"(ldsdst) : "memory");
}
DI void dma16c(unsigned voff, const char* sbase, unsigned ldsdst) {
    asm volatile("s_mov_b32 m0, %2\n\ts_nop 0\n\tglobal_load_lds_dwordx4 %0, %1 sc1" ::"v"(voff), "s"(sbase), "s"(ldsdst) : "memory");
}
DI void dma4(unsigned voff, const char* sbase, unsigned ldsdst) {
    asm volatile("s_mov_b32 m0, %2\n\ts_nop 0\n\tglobal_load_lds_dword %0, %1" ::"v"(voff), "s"(sbase), "s"(ldsdst) : "memory");
}
DI bf16x8 lds128(unsigned a) { return *(const LAS bf16x8*)(size_t)a; }
DI f32x4 ldsf4(unsigned a) { return *(const LAS f32x4*)(size_t)a; }
DI s16x4 ldstr(unsigned a) { return __builtin_amdgcn_ds_read_tr16_b64_v4i16((LAS s16x4*)(size_t)a); }
DI bf16x8 trfrag(unsigned a0, unsigned a1) { s16x4 lo = ldstr(a0), hi = ldstr(a1); return __builtin_shufflevector(lo, hi, 0, 1, 2, 3, 4, 5, 6, 7); }
DI unsigned pk2(float a, float b) { f32x2 p = {a, b}; return __builtin_bit_cast(unsigned, __builtin_convertvector(p, b16x2)); }
DI float rbf(float a) { return __uint_as_float(pk2(a, 0.f) << 16); }
DI float bflo(unsigned u) { return __uint_as_float(u << 16); }
DI float bfhi(unsigned u) { return __uint_as_float(u & 0xffff0000u); }
DI float dot2(unsigned a, unsigned b, float c) { return __builtin_amdgcn_fdot2_f32_bf16(__builtin_bit_cast(b16x2, a), __builtin_bit_cast(b16x2, b), c, false); }
DI float sum8(bf16x8 f, float c) { u32x4 u = __builtin_bit_cast(u32x4, f); unsigned a = u[0], b = u[1], d = u[2], e = u[3];
    c = dot2(a, 0x3f803f80u, c); c = dot2(b, 0x3f803f80u, c); c = dot2(d, 0x3f803f80u, c); return dot2(e, 0x3f803f80u, c); }
DI float sq8(bf16x8 f, float c) { u32x4 u = __builtin_bit_cast(u32x4, f); unsigned a = u[0], b = u[1], d = u[2], e = u[3];
    c = dot2(a, a, c); c = dot2(b, b, c); c = dot2(d, d, c); return dot2(e, e, c); }
DI float redsum4(float v) {
    auto a = __builtin_amdgcn_permlane16_swap(__float_as_uint(v), __float_as_uint(v), false, false); v = __uint_as_float(a[0]) + __uint_as_float(a[1]);
    auto b = __builtin_amdgcn_permlane32_swap(__float_as_uint(v), __float_as_uint(v), false, false); return __uint_as_float(b[0]) + __uint_as_float(b[1]); }
DI float redmax4(float v) {
    auto a = __builtin_amdgcn_permlane16_swap(__float_as_uint(v), __float_as_uint(v), false, false); v = __builtin_fmaxf(__uint_as_float(a[0]), __uint_as_float(a[1]));
    auto b = __builtin_amdgcn_permlane32_swap(__float_as_uint(v), __float_as_uint(v), false, false); return __builtin_fmaxf(__uint_as_float(b[0]), __uint_as_float(b[1])); }
DI u64 mix64(u64 z) { z += 0x9E3779B97F4A7C15ull; z = (z ^ (z >> 30)) * 0xBF58476D1CE4E5B9ull; z = (z ^ (z >> 27)) * 0x94D049BB133111EBull; return z ^ (z >> 31); }
DI void put_flag(char* ws, unsigned off, u64 ep) { __hip_atomic_store((GAS u64*)(ws + off), ep, RLX_AGENT); }
DI u64 get_flag(char* ws, unsigned off) { return __hip_atomic_load((GAS u64*)(ws + off), RLX_AGENT); }
DI void spin_flags(char* ws, unsigned off, bool need, u64 ep) {
#pragma nounroll
    for (unsigned it = 0; it < SPIN_MAX; ++it) {
        const bool ok = get_flag(ws, off) == ep || !need;
        if (__all(ok)) break;
        __builtin_amdgcn_s_sleep(1);
    }
}

#define STAMP(i) do { } while (0)

#define DIAGARG
struct P1Lane { unsigned xa[2], wa[2][2]; };

template <bool B1Q>
DI void pass_a(bf16x8 (&xf)[7][2][2], f32x4 (&acc)[2][2][2], float (&ssum)[2], float (&ssq)[2], float (&cs)[2][2], const P1Lane& L, int slab,
               const char* xs, const char* w0s, const char* w1s, const char* w2s, unsigned p0, unsigned p1, unsigned p2, int c0,
               unsigned xv, unsigned wv0, unsigned wv1, unsigned wv2, unsigned wk
) {
#pragma unroll
    for (int i = 0; i < 7; ++i) {
        if (i == 0) WAITBAR(15); else if (i < 5) WAITBAR(11); else if (i == 5) WAITBAR(11); else WAITBAR(7);
        if (i == 0) STAMP(13);
        if (i == 3) STAMP(14);
        if (i >= 1 && i <= 4) {
#pragma unroll
            for (int n = 19 + 4 * (i - 1); n < 23 + 4 * (i - 1); ++n) {
                if (n < 28) {
                    const int ci = n >> 2, ty = n & 3; int kc = c0 + ci; kc = kc >= 7 ? kc - 7 : kc;
                    if (ty < 2) dma16(xv, xs + ty * (64 * 896) + kc * 128, phys(n) + wk);
                    else if (ty == 2) dma16(wv0, w0s + (size_t)kc * 64 * p0, phys(n) + wk);
                    else dma16(wv1, w1s + (size_t)kc * 64 * p1, phys(n) + wk);
                } else {
                    int kc = c0 + (n - 28); kc = kc >= 7 ? kc - 7 : kc;
                    dma16(wv2, w2s + (size_t)kc * 64 * p2, phys(n) + wk);
                }
            }
        }
#pragma unroll
        for (int e = 0; e < 2; ++e) {
#pragma unroll
            for (int tt = 0; tt < 2; ++tt) xf[i][e][tt] = lds128(L.xa[e] + phys(4 * i) + tt * 2048);
            bf16x8 wf[2][2];
#pragma unroll
            for (int b = 0; b < 2; ++b)
#pragma unroll
                for (int nt = 0; nt < 2; ++nt) wf[b][nt] = trfrag(L.wa[e][nt] + phys(4 * i + 2 + b), L.wa[e][nt] + phys(4 * i + 2 + b) + 512);
#pragma unroll
            for (int nt = 0; nt < 2; ++nt)
#pragma unroll
                for (int tt = 0; tt < 2; ++tt) {
                    acc[0][nt][tt] = MFMA(xf[i][e][tt], wf[0][nt], acc[0][nt][tt]);
                    acc[1][nt][tt] = B1Q ? MFMA(xf[i][e][tt], wf[1][nt], acc[1][nt][tt]) : MFMA(wf[1][nt], xf[i][e][tt], acc[1][nt][tt]);
                }
            if (e == 0) {
#pragma unroll
                for (int tt = 0; tt < 2; ++tt) { ssum[tt] = sum8(xf[i][0][tt], ssum[tt]); ssq[tt] = sq8(xf[i][0][tt], ssq[tt]); }
            }
            if (((2 * i + e) & 3) == slab) {
#pragma unroll
                for (int b = 0; b < 2; ++b)
#pragma unroll
                    for (int nt = 0; nt < 2; ++nt) cs[b][nt] = sum8(wf[b][nt], cs[b][nt]);
            }
        }
    }
}

DI void epi_qkv(const f32x4 (&acc)[2][2], int csslot, int blk, int ch, int tb, int lane, int wv, __amdgpu_buffer_rsrc_t rs) {
    const int r = lane & 15, g = lane >> 4;
    f32x4 st[2][2];
#pragma unroll
    for (int tt = 0; tt < 2; ++tt) { st[tt][0] = ldsf4(L_ST + wv * 256 + (16 * tt + 4 * g) * 8); st[tt][1] = ldsf4(L_ST + wv * 256 + (16 * tt + 4 * g) * 8 + 16); }
#pragma unroll
    for (int nt = 0; nt < 2; ++nt) {
        const int nl = 32 * ch + 16 * nt + r;
        const f32x4 c4 = ldsf4(L_CS + csslot * 1024 + nl * 16);
        const float cs = (c4[0] + c4[1]) + (c4[2] + c4[3]);
        const unsigned n = 64 * blk + nl, sec = n / 448u, rr = n - sec * 448u, hd = rr / 28u, d = rr - hd * 28u;
        const float qs = sec == 0 ? 0.27264391f   : 1.f;
        unsigned w[2][2];
#pragma unroll
        for (int tt = 0; tt < 2; ++tt) {
            const float v0 = (acc[nt][tt][0] - st[tt][0][0] * cs) * (st[tt][0][1] * qs), v1 = (acc[nt][tt][1] - st[tt][0][2] * cs) * (st[tt][0][3] * qs);
            const float v2 = (acc[nt][tt][2] - st[tt][1][0] * cs) * (st[tt][1][1] * qs), v3 = (acc[nt][tt][3] - st[tt][1][2] * cs) * (st[tt][1][3] * qs);
            w[tt][0] = pk2(v0, v1); w[tt][1] = pk2(v2, v3);
        }
        const bool sw = sec < 2 && (d & 8);
        const u32x4 o = {sw ? w[1][0] : w[0][0], sw ? w[1][1] : w[0][1], sw ? w[0][0] : w[1][0], sw ? w[0][1] : w[1][1]};
        const unsigned off = sec < 2 ? WS_Q + sec * (WS_K - WS_Q) + (hd * 64 + tb) * 1792 + g * 448 + d * 16 : WS_V + (hd * 64 + tb) * 2048 + g * 512 + d * 16;
        __builtin_amdgcn_raw_buffer_store_b128(o, rs, off, 0, 16);
    }
}

DI void epi_ffn_math(const f32x4 (&acc)[2][2], u32x4 (&o)[2], int csslot, int b1slot, int ch, int lane, const float (&mean)[2], const float (&rstd)[2]) {
    const int g = lane >> 4;
    float cs[2][4], bb[2][4];
#pragma unroll
    for (int nt = 0; nt < 2; ++nt) {
        const int nl = 32 * ch + 16 * nt + 4 * g;
#pragma unroll
        for (int j = 0; j < 4; ++j) { const f32x4 c4 = ldsf4(L_CS + csslot * 1024 + (nl + j) * 16); cs[nt][j] = (c4[0] + c4[1]) + (c4[2] + c4[3]); }
        const u32x2 b = *(const LAS u32x2*)(size_t)(L_B1 + b1slot * 128 + nl * 2);
        bb[nt][0] = bflo(b[0]); bb[nt][1] = bfhi(b[0]); bb[nt][2] = bflo(b[1]); bb[nt][3] = bfhi(b[1]);
    }
#pragma unroll
    for (int tt = 0; tt < 2; ++tt) {
        float v[2][4];
#pragma unroll
        for (int nt = 0; nt < 2; ++nt)
#pragma unroll
            for (int j = 0; j < 4; ++j) { float u = (acc[nt][tt][j] - mean[tt] * cs[nt][j]) * rstd[tt] + bb[nt][j]; u = __builtin_fmaxf(u, 0.f); v[nt][j] = u * u; }
        o[tt] = (u32x4){pk2(v[0][0], v[0][1]), pk2(v[0][2], v[0][3]), pk2(v[1][0], v[1][1]), pk2(v[1][2], v[1][3])};
    }
}

__global__ void __launch_bounds__(512, 2) fused_k(const char* __restrict__ x, const char* __restrict__ wqkv, const char* __restrict__ wo, const char* __restrict__ w1,
                                                  const char* __restrict__ b1, const char* __restrict__ w2, const char* __restrict__ b2, char* __restrict__ out, char* ws) {
    STAMP(0);
    const int tid = threadIdx.x, lane = tid & 63, wv = __builtin_amdgcn_readfirstlane(tid >> 6);
    const int r16 = lane & 15, g = lane >> 4, q4 = (lane & 15) >> 2, p4 = lane & 3;
    const int bid = blockIdx.x, xcd = bid & 7, idx = bid >> 3;
    const unsigned wk = wv * 1024;
    const int rt = (xcd & 1) * 8 + (idx & 7), G = xcd >> 1, slot = G * 4 + (idx >> 3);
    const bool two = slot < 5;
    const int qb0 = two ? 2 * slot : slot + 5;
    const int blk1 = two ? 2 * slot + 1 : 2 * slot - 5;
    const int fb2 = two ? slot : 2 * slot - 4;
    const bool spare = G == 0 ? (idx >= 24 && idx < 28) : idx >= 28;
    const bool extra = spare && G < 2;
    const int p3 = (G == 0 && idx >= 28) ? idx - 4 : (spare ? 0 : idx);
    const int rt64 = 4 * xcd + p3 / 7, cb = p3 % 7;
    const int hd = 2 * xcd + (idx & 1), art = idx >> 1;
    const int slab = wv & 3, ch = wv >> 2;
    const int c0 = (rt + slot) % 7;
    const u64 ep = mix64(*(const u64*)(ws + WS_E));
    const __amdgpu_buffer_rsrc_t rs = __builtin_amdgcn_make_buffer_rsrc(ws, 0, (int)WS_END, 0x00020000);

    const char* xs = x + (size_t)rt * 128 * 896;
    const char* w0s = wqkv + qb0 * 128;
    const char* w1s = two ? wqkv + blk1 * 128 : w1 + blk1 * 128;
    const char* w2s = w1 + fb2 * 128;
    const unsigned p0 = 2688, p1 = two ? 2688 : 3584, p2 = 3584;
    const int drow = 8 * wv + (lane >> 3), dc = lane & 7;
    const unsigned xv = drow * 896 + ((dc ^ ((drow >> 1) & 7)) << 4);
    const unsigned wsw1 = (dc ^ ((((drow >> 1) & 1) | (((drow >> 3) & 1) << 1)) << 1)) << 4;
    const unsigned wv0 = drow * p0 + wsw1, wv1 = drow * p1 + wsw1, wv2 = drow * p2 + wsw1;
    if (wv < 4 && lane < 32) {
        const char* src = wv == 0 ? b1 + (two ? 0 : blk1) * 128 : wv == 1 ? b1 + fb2 * 128 : wv == 2 ? b1 + 27 * 128 : b2 + cb * 128;
        dma4(lane * 4, src, L_B1 + wv * 128);
    }
#pragma unroll
    for (int n = 0; n < 19; ++n) {
        const int ci = n >> 2, ty = n & 3; int kc = c0 + ci; kc = kc >= 7 ? kc - 7 : kc;
        if (ty < 2) dma16(xv, xs + ty * (64 * 896) + kc * 128, phys(n) + wk);
        else if (ty == 2) dma16(wv0, w0s + (size_t)kc * 64 * p0, phys(n) + wk);
        else dma16(wv1, w1s + (size_t)kc * 64 * p1, phys(n) + wk);
    }
    STAMP(12);
    P1Lane L;
    {
        const int fl = r16 >> 1, key = ((q4 >> 1) & 1) | ((g & 1) << 1);
#pragma unroll
        for (int e = 0; e < 2; ++e) {
            const int ee = e ^ ch;
            L.xa[e] = (slab >> 1) * UNIT + ((slab & 1) * 32 + r16) * 128 + (((4 * ee + g) ^ fl) << 4);
#pragma unroll
            for (int nt = 0; nt < 2; ++nt) L.wa[e][nt] = ee * 4096 + (8 * g + q4) * 128 + (((2 * ch + nt) ^ key) << 5) + p4 * 8;
        }
    }
    bf16x8 xf[7][2][2];
    f32x4 acc[2][2][2];
    float ssum[2] = {0.f, 0.f}, ssq[2] = {0.f, 0.f}, cs[2][2] = {{0.f, 0.f}, {0.f, 0.f}};
#pragma unroll
    for (int b = 0; b < 2; ++b)
#pragma unroll
        for (int nt = 0; nt < 2; ++nt)
#pragma unroll
            for (int tt = 0; tt < 2; ++tt) acc[b][nt][tt] = (f32x4){0.f, 0.f, 0.f, 0.f};
    if (two) pass_a<true>(xf, acc, ssum, ssq, cs, L, slab, xs, w0s, w1s, w2s, p0, p1, p2, c0, xv, wv0, wv1, wv2, wk DIAGARG);
    else pass_a<false>(xf, acc, ssum, ssq, cs, L, slab, xs, w0s, w1s, w2s, p0, p1, p2, c0, xv, wv0, wv1, wv2, wk DIAGARG);
    STAMP(1);
#pragma unroll
    for (int tt = 0; tt < 2; ++tt) {
        const float a = redsum4(ssum[tt]), b = redsum4(ssq[tt]);
        if (g == 0) *(LAS f32x2*)(size_t)(L_RS + ch * 1024 + (32 * slab + 16 * tt + r16) * 8) = (f32x2){a, b};
    }
#pragma unroll
    for (int b = 0; b < 2; ++b)
#pragma unroll
        for (int nt = 0; nt < 2; ++nt) {
            const float a = redsum4(cs[b][nt]);
            if (g == 0) *(LAS float*)(size_t)(L_CS + b * 1024 + (32 * ch + 16 * nt + r16) * 16 + slab * 4) = a;
        }
    BAR();
    float mean[2], rstd[2];
#pragma unroll
    for (int tt = 0; tt < 2; ++tt) {
        const int tok = 32 * slab + 16 * tt + r16;
        const f32x2 a = *(const LAS f32x2*)(size_t)(L_RS + tok * 8), b = *(const LAS f32x2*)(size_t)(L_RS + 1024 + tok * 8);
        const float m = (a[0] + b[0]) * (1.f / 448.f);
        const float var = __builtin_fmaxf((a[1] + b[1]) * (1.f / 448.f) - m * m, 0.f);
        const float sd = rbf(__builtin_sqrtf(rbf(rbf(var) + 1e-5f)));
        mean[tt] = rbf(m); rstd[tt] = 1.f / sd;
        if (g == 0) *(LAS f32x2*)(size_t)(L_ST + wv * 256 + (16 * tt + r16) * 8) = (f32x2){mean[tt], rstd[tt]};
    }
    const int tb = 4 * rt + slab;
    epi_qkv(acc[0], 0, qb0, ch, tb, lane, wv, rs);
    if (two) epi_qkv(acc[1], 1, blk1, ch, tb, lane, wv, rs);
    else {
        u32x4 o[2]; epi_ffn_math(acc[1], o, 1, 0, ch, lane, mean, rstd);
#pragma unroll
        for (int tt = 0; tt < 2; ++tt) __builtin_amdgcn_raw_buffer_store_b128(o[tt], rs, WS_H + ((8 * rt + 2 * slab + tt) * 56 + 2 * blk1 + ch) * 1024 + lane * 16, 0, 16);
    }
    STAMP(2);
    const int r0 = (rt64 * 5 + (xcd >> 1)) % 13;
    const unsigned wsw3 = (dc ^ (((drow >> 1) & 3) << 1)) << 4;
    const unsigned wv3 = drow * 896 + wsw3;
    const char* w2b = w2 + cb * 128;
    if (extra) {
#pragma unroll
        for (int m = 0; m < 12; ++m) { int kc = c0 + (m < 7 ? m : 0); kc = kc >= 7 ? kc - 7 : kc; dma16(wv2, w1 + 27 * 128 + (size_t)kc * 64 * 3584, m * UNIT + wk); }
    } else {
#pragma unroll
        for (int m = 0; m < 12; ++m) { int jj = r0 + (m >> 1); jj = jj >= 13 ? jj - 13 : jj; dma16(wv3, w2b + (size_t)((m & 1) * 896 + jj * 64) * 896, m * UNIT + wk); }
    }
    const unsigned fa0 = WS_FQ + ((2 * qb0 + ch) * 64 + tb) * 8;
    const unsigned fa1 = two ? WS_FQ + ((2 * blk1 + ch) * 64 + tb) * 8 : WS_FH + (((rt * 4 + slab) * 28 + blk1) * 2 + ch) * 8;
    int fb = fb2;
    const int nit = extra ? 2 : 1;
    for (int it = 0; it < nit; ++it) {
        if (it == 0) WAITBAR(16); else WAITBAR(9);
        if (it == 0) STAMP(15);
        const unsigned ub = it == 0 ? L_REG : 0;
        f32x4 ac[2][2]; float c2[2] = {0.f, 0.f};
#pragma unroll
        for (int nt = 0; nt < 2; ++nt)
#pragma unroll
            for (int tt = 0; tt < 2; ++tt) ac[nt][tt] = (f32x4){0.f, 0.f, 0.f, 0.f};
#pragma unroll
        for (int i = 0; i < 7; ++i)
#pragma unroll
            for (int e = 0; e < 2; ++e) {
                bf16x8 wf[2];
#pragma unroll
                for (int nt = 0; nt < 2; ++nt) wf[nt] = trfrag(L.wa[e][nt] + ub + i * UNIT, L.wa[e][nt] + ub + i * UNIT + 512);
#pragma unroll
                for (int nt = 0; nt < 2; ++nt)
#pragma unroll
                    for (int tt = 0; tt < 2; ++tt) ac[nt][tt] = MFMA(wf[nt], xf[i][e][tt], ac[nt][tt]);
                if (((2 * i + e) & 3) == slab) { c2[0] = sum8(wf[0], c2[0]); c2[1] = sum8(wf[1], c2[1]); }
            }
        if (it == 0) {
            WAITV(12);
            if (lane == 0) { put_flag(ws, fa0, ep); put_flag(ws, fa1, ep); }
        } else {
            WAITV(0);
            if (lane == 0) put_flag(ws, WS_FH + (((rt * 4 + slab) * 28 + fb2) * 2 + ch) * 8, ep);
            fb = 27;
        }
#pragma unroll
        for (int nt = 0; nt < 2; ++nt) {
            const float a = redsum4(c2[nt]);
            if (g == 0) *(LAS float*)(size_t)(L_CS + it * 1024 + (32 * ch + 16 * nt + r16) * 16 + slab * 4) = a;
        }
        BAR();
        u32x4 o[2]; epi_ffn_math(ac, o, it, 1 + it, ch, lane, mean, rstd);
        if (it == nit - 1) {
            STAMP(3);
            if (wv == 0) {
                const int sel = lane >= 28, m = lane - 28 * sel;
                const int sec = m < 4 ? 0 : (m < 16 ? 1 : 2);
                const int t = m < 4 ? 4 * art + m : (m < 16 ? 4 * art - 12 + m : 4 * art - 24 + m);
                const int st = 448 * sec + 28 * hd, un = (st + 27 * sel) >> 5;
                spin_flags(ws, WS_FQ + ((lane < 56 ? un : 0) * 64 + (t < 0 ? 0 : t)) * 8, lane < 56 && t >= 0, ep);
            }
        }
#pragma unroll
        for (int tt = 0; tt < 2; ++tt) __builtin_amdgcn_raw_buffer_store_b128(o[tt], rs, WS_H + ((8 * rt + 2 * slab + tt) * 56 + 2 * fb + ch) * 1024 + lane * 16, 0, 16);
    }
    if (tid < 48) *(LAS u32x4*)(size_t)(L_V + (tid >> 2) * 2048 + (tid & 3) * 512 + 28 * 16) = (u32x4){0x3f803f80u, 0x3f803f80u, 0x3f803f80u, 0x3f803f80u};
    BAR();
    STAMP(4);
    {
        const int nskK = art < 2 ? 14 - 7 * art : 0, nskV = art < 2 ? 16 - 8 * art : 0;
        const char* qs = ws + WS_Q + (hd * 64 + 4 * art) * 1792;
        const char* ks = ws + WS_K + (hd * 64 + 4 * art - 8) * 1792;
        const char* vs = ws + WS_V + (hd * 64 + 4 * art - 8) * 2048;
#pragma unroll
        for (int i = 0; i < 7; ++i) {
            const int n = wv + 8 * i;
            const char* src = n < 7 ? qs + n * 1024 : (n < 28 ? (n - 7 < nskK ? x : ks + (n - 7) * 1024) : ((n - 28 < nskV || n >= 52) ? x : vs + (n - 28) * 1024));
            if (n < 28 || (lane & 31) < 28) dma16c(lane * 16, src, L_REG + n * 1024);
        }
    }
    WAITV(7);
    if (lane == 0) put_flag(ws, WS_FH + (((rt * 4 + slab) * 28 + fb) * 2 + ch) * 8, ep);
    WAITBAR(1);
    STAMP(5);
    const int tt3 = wv & 3, kh = wv >> 2;
    const unsigned fhoff = WS_FH + ((((rt64 >> 1) * 4 + 2 * (rt64 & 1) + (tt3 >> 1)) * 28 + 14 * kh) * 2) * 8;
    const bool hneed = lane < 28 - 2 * kh;
    const u32x4 xres = *(const GAS u32x4*)(x + (size_t)(rt64 * 64 + (tid >> 3)) * 896 + cb * 128 + (tid & 7) * 16);
    u64 hfl = get_flag(ws, fhoff + (hneed ? lane : 0) * 8);
    __builtin_amdgcn_sched_barrier(0);
    u32x4 pf[9];
    {
        const unsigned qa = L_Q + (wv >> 1) * 1792 + p4 * 448 + (8 * g + q4) * 16 + (((wv & 1) ^ (g & 1)) << 3);
        u32x4 qf4 = __builtin_bit_cast(u32x4, trfrag(qa, qa + 64));
        if (g == 3) { qf4[2] = 0; qf4[3] = 0; }
        const bf16x8 qf = __builtin_bit_cast(bf16x8, qf4);
        const unsigned ka0 = L_K + (wv >> 1) * 1792 + p4 * 448 + (8 * g + q4) * 16 + ((g & 1) << 3), ka1 = ka0 ^ 8;
        f32x4 s[18];
#pragma unroll
        for (int t = 0; t < 18; ++t) {
            const unsigned a = ((t & 1) ? ka1 : ka0) + (t >> 1) * 1792;
            s[t] = MFMA(trfrag(a, a + 64), qf, ((f32x4){0.f, 0.f, 0.f, 0.f}));
        }
        const int dq = 256 + 16 * (wv & 1) + r16 - 4 * g;
#pragma unroll
        for (int t = 0; t < 18; ++t) {
            if (t < 2 || t >= 16) {
#pragma unroll
                for (int j = 0; j < 4; ++j) { const unsigned dd = dq - 16 * t - j; s[t][j] = dd < 256u ? s[t][j] : -INFINITY; }
            }
        }
        if (art < 2) {
            const int kmin = 16 - 8 * art - 2 * (wv >> 1);
#pragma unroll
            for (int t = 0; t < 16; ++t) if (t < kmin) s[t] = (f32x4){-INFINITY, -INFINITY, -INFINITY, -INFINITY};
        }
        float m0 = __builtin_fmaxf(s[0][0], s[0][1]), m1 = __builtin_fmaxf(s[0][2], s[0][3]);
#pragma unroll
        for (int t = 1; t < 18; ++t) { m0 = __builtin_fmaxf(__builtin_fmaxf(m0, s[t][0]), s[t][1]); m1 = __builtin_fmaxf(__builtin_fmaxf(m1, s[t][2]), s[t][3]); }
        const float mx = redmax4(__builtin_fmaxf(m0, m1));
#pragma unroll
        for (int b = 0; b < 9; ++b) {
            float e[8];
#pragma unroll
            for (int j = 0; j < 8; ++j) e[j] = __builtin_amdgcn_exp2f(s[2 * b + (j >> 2)][j & 3] - mx);
            pf[b] = (u32x4){pk2(e[0], e[1]), pk2(e[2], e[3]), pk2(e[4], e[5]), pk2(e[6], e[7])};
        }
    }
    STAMP(6);
    u32x4 hf[28];
    const unsigned hbase = WS_H + ((4 * rt64 + tt3) * 56 + 28 * kh) * 1024 + lane * 16;
#define HOFF(i) (hbase + (((i) >> 1) == 13 ? 13 : ((r0 + ((i) >> 1)) >= 13 ? r0 + ((i) >> 1) - 13 : r0 + ((i) >> 1))) * 2048 + ((i) & 1) * 1024)
    if (!spare) {
        asm volatile("" : "+v"(hfl));
        if (!__all(hfl == ep)) spin_flags(ws, fhoff + (hneed ? lane : 0) * 8, true, ep);
#pragma unroll
        for (int i = 0; i < NEARLY; ++i) hf[i] = __builtin_amdgcn_raw_buffer_load_b128(rs, HOFF(i), 0, 16);
    }
    {
        const unsigned va0 = L_V + (wv >> 1) * 2048 + g * 512 + r16 * 16;
        const unsigned va1 = L_V + (wv >> 1) * 2048 + g * 512 + (p4 == 3 ? 28 : 16 + 3 * q4 + p4) * 16;
        f32x4 o0 = {0.f, 0.f, 0.f, 0.f}, o1 = {0.f, 0.f, 0.f, 0.f};
        const int bmin = art < 2 ? 8 - 4 * art - (wv >> 1) : 0;
#pragma unroll
        for (int b = 0; b < 9; ++b) {
            if (b >= bmin) {
                o0 = MFMA(lds128(va0 + b * 2048), __builtin_bit_cast(bf16x8, pf[b]), o0);
                o1 = MFMA(lds128(va1 + b * 2048), __builtin_bit_cast(bf16x8, pf[b]), o1);
            }
        }
        const float il = __builtin_amdgcn_rcpf(o1[3]);
        const u32x4 of = {pk2(o0[0] * il, o0[1] * il), pk2(o0[2] * il, o0[3] * il), pk2(o1[0] * il, o1[1] * il), pk2(o1[2] * il, 0.f)};
        __builtin_amdgcn_raw_buffer_store_b128(of, rs, WS_O + ((8 * art + wv) * 16 + hd) * 1024 + lane * 16, 0, 16);
    }
    STAMP(7);
    const unsigned fo = WS_FO + ((8 * art + wv) * 16 + hd) * 8;
    if (spare) {
        WAITV(0);
        if (lane == 0) put_flag(ws, fo, ep);
        if (bid == 4 + 8 * 28) {
#pragma unroll
            for (int i = 0; i < 4; ++i) spin_flags(ws, WS_FO + (tid * 4 + i) * 8, true, ep);
            BAR();
            if (tid == 0) put_flag(ws, WS_E, ep);
        }
    } else {
#pragma unroll
        for (int i = NEARLY; i < 26; ++i) hf[i] = __builtin_amdgcn_raw_buffer_load_b128(rs, HOFF(i), 0, 16);
        BAR();
#pragma unroll
        for (int i = 0; i < 7; ++i) dma16(wv3, wo + cb * 128 + (size_t)i * 64 * 896, L_REG + i * UNIT + wk);
        unsigned wb[4];
        {
            const int key3 = (2 * g + (q4 >> 1)) & 3;
#pragma unroll
            for (int nt = 0; nt < 4; ++nt) wb[nt] = kh * UNIT + (4 * g + q4) * 128 + ((nt ^ key3) << 5) + p4 * 8;
        }
        f32x4 ac[4];
#pragma unroll
        for (int nt = 0; nt < 4; ++nt) ac[nt] = (f32x4){0.f, 0.f, 0.f, 0.f};
#pragma unroll
        for (int j = 0; j < 13; ++j) {
            if (j >= 1) {
                if (j < 6) BAR();
                else WAITBAR(2 * (13 - j < 4 ? 13 - j : 4) + ((OFLAG_AT >= j - 5 && OFLAG_AT < j) ? 1 : 0));
                if (j + 5 <= 13) {
                    int jj = j + 5 == 13 ? 13 : r0 + j + 5; jj = (j + 5 != 13 && jj >= 13) ? jj - 13 : jj;
                    dma16(wv3, w2b + (size_t)(jj * 64) * 896, ((2 * (j + 5)) % 12) * UNIT + wk);
                    dma16(wv3, w2b + (size_t)(896 + jj * 64) * 896, ((2 * (j + 5) + 1) % 12) * UNIT + wk);
                }
            }
#pragma unroll
            for (int e = 0; e < 2; ++e)
#pragma unroll
                for (int nt = 0; nt < 4; ++nt) {
                    const unsigned a = wb[nt] + ((2 * j) % 12) * UNIT + e * 4096;
                    ac[nt] = MFMA(trfrag(a, a + 2048), __builtin_bit_cast(bf16x8, hf[2 * j + e]), ac[nt]);
                }
            if (j == OFLAG_AT) {
                WAITV((26 - NEARLY) + 7 + 2 * OFLAG_AT);
                if (lane == 0) put_flag(ws, fo, ep);
            }
        }
        WAITBAR(0);
        STAMP(8);
        bf16x8 wl[2][4], wof[8][4];
#pragma unroll
        for (int e = 0; e < 2; ++e)
#pragma unroll
            for (int nt = 0; nt < 4; ++nt) { const unsigned a = wb[nt] + 2 * UNIT + e * 4096; wl[e][nt] = trfrag(a, a + 2048); }
        {
            const int d0 = 4 * g + q4, d1 = 16 + 3 * g + (q4 < 3 ? q4 : 2);
#pragma unroll
            for (int nt = 0; nt < 4; ++nt) {
                const unsigned a0 = L_REG + (kh * 224 + d0) * 128 + ((nt ^ ((d0 >> 1) & 3)) << 5) + p4 * 8;
                const unsigned a1 = L_REG + (kh * 224 + d1) * 128 + ((nt ^ ((d1 >> 1) & 3)) << 5) + p4 * 8;
#pragma unroll
                for (int i = 0; i < 8; ++i) wof[i][nt ^ ((i & 1) << 1)] = trfrag(a0 + i * 3584, a1 + i * 3584);
            }
        }
        {
            const unsigned foff = lane < 8 ? WS_FO + ((4 * rt64 + tt3) * 16 + 8 * kh + lane) * 8 : fhoff + (26 + (lane & 1)) * 8;
            spin_flags(ws, foff, lane < 10, ep);
        }
        STAMP(9);
        u32x4 of[8];
        hf[26] = __builtin_amdgcn_raw_buffer_load_b128(rs, HOFF(26), 0, 16);
        hf[27] = __builtin_amdgcn_raw_buffer_load_b128(rs, HOFF(27), 0, 16);
#pragma unroll
        for (int i = 0; i < 8; ++i) of[i] = __builtin_amdgcn_raw_buffer_load_b128(rs, WS_O + ((4 * rt64 + tt3) * 16 + 8 * kh + i) * 1024 + lane * 16, 0, 16);
        __builtin_amdgcn_sched_barrier(0);
#pragma unroll
        for (int e = 0; e < 2; ++e)
#pragma unroll
            for (int nt = 0; nt < 4; ++nt) ac[nt] = MFMA(wl[e][nt], __builtin_bit_cast(bf16x8, hf[26 + e]), ac[nt]);
#pragma unroll
        for (int i = 0; i < 8; ++i)
#pragma unroll
            for (int nt = 0; nt < 4; ++nt) ac[nt] = MFMA(wof[i][nt], __builtin_bit_cast(bf16x8, of[i]), ac[nt]);
#pragma unroll
        for (int nt = 0; nt < 4; ++nt) *(LAS f32x4*)(size_t)(L_RED + (kh * 64 + tt3 * 16 + r16) * RED_RS + nt * 64 + g * 16) = ac[nt];
        BAR();
        STAMP(10);
        {
            const unsigned ra = L_RED + (tid >> 3) * RED_RS + (tid & 7) * 32;
            const f32x4 a0 = ldsf4(ra), a1 = ldsf4(ra + 16), c0_ = ldsf4(ra + 64 * RED_RS), c1_ = ldsf4(ra + 64 * RED_RS + 16);
            const u32x4 bb = __builtin_bit_cast(u32x4, lds128(L_B2 + (tid & 7) * 16));
            u32x4 res;
#pragma unroll
            for (int j = 0; j < 4; ++j) {
                const unsigned xj = xres[j], bj = bb[j];
                const float s0 = j < 2 ? a0[2 * j] + c0_[2 * j] : a1[2 * j - 4] + c1_[2 * j - 4];
                const float s1 = j < 2 ? a0[2 * j + 1] + c0_[2 * j + 1] : a1[2 * j - 3] + c1_[2 * j - 3];
                res[j] = pk2(s0 + bflo(xj) + bflo(bj), s1 + bfhi(xj) + bfhi(bj));
            }
            *(GAS u32x4*)(out + (size_t)(rt64 * 64 + (tid >> 3)) * 896 + cb * 128 + (tid & 7) * 16) = res;
        }
    }
    STAMP(11);
}

extern "C" void kernel_launch(void* const* d_in, const int* in_sizes, int n_in, void* d_out, int out_size, void* d_ws, size_t ws_size, hipStream_t stream) {
    (void)in_sizes; (void)n_in; (void)out_size; (void)ws_size;
    (void)hipFuncSetAttribute((const void*)fused_k, hipFuncAttributeMaxDynamicSharedMemorySize, LDS_BYTES);
    fused_k<<<256, 512, LDS_BYTES, stream>>>((const char*)d_in[0], (const char*)d_in[1], (const char*)d_in[2], (const char*)d_in[4], (const char*)d_in[5],
                                             (const char*)d_in[6], (const char*)d_in[7], (char*)d_out, (char*)d_ws);
}
```

```cpp
#include <hip/hip_runtime.h>
#include <hip/hip_bf16.h>
#include <stdint.h>

typedef unsigned short u16;
static __device__ __forceinline__ float bf2f(u16 v) { return __uint_as_float(((unsigned)v) << 16); }
static __device__ __forceinline__ u16 f2bf(float f) { unsigned u = __float_as_uint(f); u += 0x7fffu + ((u >> 16) & 1u); return (u16)(u >> 16); }
static __device__ __forceinline__ float rbf(float f) { return bf2f(f2bf(f)); }

constexpr int S = 2048, D = 448, F = 1792, H = 16, DH = 28, W = 256;

__global__ void ln_k(const u16* x, u16* xn) {
    int row = blockIdx.x; int lane = threadIdx.x;
    float v[7]; float s = 0;
    for (int i = 0; i < 7; i++) { v[i] = bf2f(x[row * D + lane + 64 * i]); s += v[i]; }
    for (int m = 32; m >= 1; m >>= 1) s += __shfl_xor(s, m);
    float mean = s / D; float q = 0;
    for (int i = 0; i < 7; i++) { float d = v[i] - mean; q += d * d; }
    for (int m = 32; m >= 1; m >>= 1) q += __shfl_xor(q, m);
    float var = rbf(q / D); float mb = rbf(mean);
    float sd = rbf(sqrtf(rbf(var + 1e-5f)));
    for (int i = 0; i < 7; i++) xn[row * D + lane + 64 * i] = f2bf(rbf(v[i] - mb) / sd);
}
__global__ void gemm_k(const u16* A, const u16* B, const u16* bias, u16* C, int N, int K, int mode) {
    int col = blockIdx.x * 64 + threadIdx.x; int row = blockIdx.y;
    if (col >= N) return;
    float acc = 0;
    for (int k = 0; k < K; k++) acc += bf2f(A[row * K + k]) * bf2f(B[k * N + col]);
    acc = rbf(acc);
    if (mode == 1) { acc = rbf(acc + bf2f(bias[col])); acc = acc > 0 ? acc : 0; acc = acc * acc; }
    if (mode == 2) { acc = rbf(acc + bf2f(bias[col])); }
    C[row * N + col] = f2bf(acc);
}
__global__ void attn_k(const u16* qkv, u16* o) {
    int row = blockIdx.x, h = blockIdx.y; int t = threadIdx.x;
    __shared__ float p[256]; __shared__ float red[4];
    int key = row - 255 + t; float s = -1e30f;
    if (key >= 0) { float a = 0; for (int d = 0; d < DH; d++) a += bf2f(qkv[row * 1344 + h * DH + d]) * bf2f(qkv[key * 1344 + 448 + h * DH + d]); s = rbf(rbf(a) * 0.1889822365f); }
    float m = s; for (int k = 32; k >= 1; k >>= 1) m = fmaxf(m, __shfl_xor(m, k));
    if ((t & 63) == 0) red[t >> 6] = m; __syncthreads();
    m = fmaxf(fmaxf(red[0], red[1]), fmaxf(red[2], red[3])); __syncthreads();
    float e = key >= 0 ? __expf(s - m) : 0.f; float l = e; for (int k = 32; k >= 1; k >>= 1) l += __shfl_xor(l, k);
    if ((t & 63) == 0) red[t >> 6] = l; __syncthreads();
    l = red[0] + red[1] + red[2] + red[3];
    p[t] = rbf(e / l); __syncthreads();
    if (t < DH) { float a = 0; for (int j = 0; j < 256; j++) { int kk = row - 255 + j; if (kk >= 0) a += p[j] * bf2f(qkv[kk * 1344 + 896 + h * DH + t]); } o[row * D + h * DH + t] = f2bf(a); }
}
__global__ void fin_k(const u16* x, const u16* a, const u16* b, u16* out) {
    int i = blockIdx.x * 256 + threadIdx.x;
    out[i] = f2bf(rbf(bf2f(x[i]) + bf2f(a[i])) + bf2f(b[i]));
}


typedef unsigned u32x4 __attribute__((ext_vector_type(4)));
__device__ __forceinline__ unsigned long long rt() { return __builtin_amdgcn_s_memrealtime(); }
__global__ void __launch_bounds__(512) probe(unsigned* sink, int iters) {
    unsigned a = threadIdx.x, b = a * 3, c = a * 5, d = a * 7;
    unsigned long long dt0 = 0, dt1 = 0;
    unsigned long long ts = rt();
    {
        unsigned long long pc; asm volatile("s_getpc_b64 %0" : "=s"(pc));
        const u32x4* p = (const u32x4*)(pc & ~255ull);
        u32x4 acc = {0, 0, 0, 0};
        if (threadIdx.x < 128) acc = p[(blockIdx.x >> 3) * 64 + threadIdx.x];
        a ^= (acc[0] ^ acc[1] ^ acc[2] ^ acc[3]) == 0x1234567;
        __syncthreads();
        dt1 = rt() - ts;
        while (rt() < ts + 0) __builtin_amdgcn_s_sleep(1);
    }
    unsigned long long pre = dt1;
#pragma nounroll
    for (int it = 0; it < iters; ++it) {
        unsigned long long t0 = __builtin_amdgcn_s_memtime();
        asm volatile("s_waitcnt lgkmcnt(0)");
        asm volatile(".rept %4\n\tv_add3_u32 %0, %0, 1, %1\n\tv_add3_u32 %1, %1, 1, %2\n\tv_add3_u32 %2, %2, 1, %3\n\tv_add3_u32 %3, %3, 1, %0\n\t.endr" : "+v"(a), "+v"(b), "+v"(c), "+v"(d) : "i"(32 * 32));
        unsigned long long t1 = __builtin_amdgcn_s_memtime();
        asm volatile("s_waitcnt lgkmcnt(0)");
        if (it == 0) dt0 = t1 - t0; else dt1 = t1 - t0;
    }
    if (a + b + c + d == 0x12345) sink[0] = 1;
    if (blockIdx.x == 100) { unsigned long long until = rt() + pre * 500; while (rt() < until) __builtin_amdgcn_s_sleep(8); }
}

extern "C" void kernel_launch(void* const* d_in, const int* in_sizes, int n_in, void* d_out, int out_size, void* d_ws, size_t ws_size, hipStream_t stream) {
    const u16* x = (const u16*)d_in[0]; const u16* wqkv = (const u16*)d_in[1]; const u16* wo = (const u16*)d_in[2];
    const u16* w1 = (const u16*)d_in[4]; const u16* b1 = (const u16*)d_in[5]; const u16* w2 = (const u16*)d_in[6]; const u16* b2 = (const u16*)d_in[7];
    u16* ws = (u16*)d_ws;
    u16* xn = ws; u16* qkv = xn + S * D; u16* h = qkv + S * 1344; u16* o = h + S * F; u16* a = o + S * D; u16* b = a + S * D;
    probe<<<256, 512, 0, stream>>>((unsigned*)((char*)d_ws + (200 << 20)), 2);
    ln_k<<<S, 64, 0, stream>>>(x, xn);
    gemm_k<<<dim3(21, S), 64, 0, stream>>>(xn, wqkv, nullptr, qkv, 1344, D, 0);
    gemm_k<<<dim3(28, S), 64, 0, stream>>>(xn, w1, b1, h, F, D, 1);
    attn_k<<<dim3(S, H), 256, 0, stream>>>(qkv, o);
    gemm_k<<<dim3(7, S), 64, 0, stream>>>(o, wo, nullptr, a, D, D, 0);
    gemm_k<<<dim3(7, S), 64, 0, stream>>>(h, w2, b2, b, D, F, 2);
    fin_k<<<S * D / 256, 256, 0, stream>>>(x, a, b, (u16*)d_out);
}
```

```cpp
#include <hip/hip_runtime.h>
#include <hip/hip_bf16.h>
#include <stdint.h>

typedef unsigned short u16;
static __device__ __forceinline__ float bf2f(u16 v) { return __uint_as_float(((unsigned)v) << 16); }
static __device__ __forceinline__ u16 f2bf(float f) { unsigned u = __float_as_uint(f); u += 0x7fffu + ((u >> 16) & 1u); return (u16)(u >> 16); }
static __device__ __forceinline__ float rbf(float f) { return bf2f(f2bf(f)); }

constexpr int S = 2048, D = 448, F = 1792, H = 16, DH = 28, W = 256;

__global__ void ln_k(const u16* x, u16* xn) {
    int row = blockIdx.x; int lane = threadIdx.x;
    float v[7]; float s = 0;
    for (int i = 0; i < 7; i++) { v[i] = bf2f(x[row * D + lane + 64 * i]); s += v[i]; }
    for (int m = 32; m >= 1; m >>= 1) s += __shfl_xor(s, m);
    float mean = s / D; float q = 0;
    for (int i = 0; i < 7; i++) { float d = v[i] - mean; q += d * d; }
    for (int m = 32; m >= 1; m >>= 1) q += __shfl_xor(q, m);
    float var = rbf(q / D); float mb = rbf(mean);
    float sd = rbf(sqrtf(rbf(var + 1e-5f)));
    for (int i = 0; i < 7; i++) xn[row * D + lane + 64 * i] = f2bf(rbf(v[i] - mb) / sd);
}
__global__ void gemm_k(const u16* A, const u16* B, const u16* bias, u16* C, int N, int K, int mode) {
    int col = blockIdx.x * 64 + threadIdx.x; int row = blockIdx.y;
    if (col >= N) return;
    float acc = 0;
    for (int k = 0; k < K; k++) acc += bf2f(A[row * K + k]) * bf2f(B[k * N + col]);
    acc = rbf(acc);
    if (mode == 1) { acc = rbf(acc + bf2f(bias[col])); acc = acc > 0 ? acc : 0; acc = acc * acc; }
    if (mode == 2) { acc = rbf(acc + bf2f(bias[col])); }
    C[row * N + col] = f2bf(acc);
}
__global__ void attn_k(const u16* qkv, u16* o) {
    int row = blockIdx.x, h = blockIdx.y; int t = threadIdx.x;
    __shared__ float p[256]; __shared__ float red[4];
    int key = row - 255 + t; float s = -1e30f;
    if (key >= 0) { float a = 0; for (int d = 0; d < DH; d++) a += bf2f(qkv[row * 1344 + h * DH + d]) * bf2f(qkv[key * 1344 + 448 + h * DH + d]); s = rbf(rbf(a) * 0.1889822365f); }
    float m = s; for (int k = 32; k >= 1; k >>= 1) m = fmaxf(m, __shfl_xor(m, k));
    if ((t & 63) == 0) red[t >> 6] = m; __syncthreads();
    m = fmaxf(fmaxf(red[0], red[1]), fmaxf(red[2], red[3])); __syncthreads();
    float e = key >= 0 ? __expf(s - m) : 0.f; float l = e; for (int k = 32; k >= 1; k >>= 1) l += __shfl_xor(l, k);
    if ((t & 63) == 0) red[t >> 6] = l; __syncthreads();
    l = red[0] + red[1] + red[2] + red[3];
    p[t] = rbf(e / l); __syncthreads();
    if (t < DH) { float a = 0; for (int j = 0; j < 256; j++) { int kk = row - 255 + j; if (kk >= 0) a += p[j] * bf2f(qkv[kk * 1344 + 896 + h * DH + t]); } o[row * D + h * DH + t] = f2bf(a); }
}
__global__ void fin_k(const u16* x, const u16* a, const u16* b, u16* out) {
    int i = blockIdx.x * 256 + threadIdx.x;
    out[i] = f2bf(rbf(bf2f(x[i]) + bf2f(a[i])) + bf2f(b[i]));
}


__device__ __forceinline__ unsigned long long rt() { return __builtin_amdgcn_s_memrealtime(); }
__global__ void __launch_bounds__(512) probe(unsigned* sink, int iters) {
    unsigned a = threadIdx.x, b = a * 3, c = a * 5, d = a * 7;
    unsigned long long dt0 = 0, dt1 = 0;
    const int wv = __builtin_amdgcn_readfirstlane(threadIdx.x >> 6);
    unsigned long long w0 = __builtin_amdgcn_s_memtime();
    asm volatile("s_waitcnt lgkmcnt(0)");
    {
        unsigned long long base; unsigned rel;
        asm volatile("s_getpc_b64 %0\n\tPL0:\n\ts_mov_b32 %1, PREG-PL0" : "=s"(base), "=s"(rel));
        base += rel;
#pragma nounroll
        for (int k = wv; k < 32768 / 512; k += 8) {
            unsigned long long tgt = base + (unsigned)(k * 512 + 4);
            asm volatile("s_swappc_b64 s[90:91], %0" :: "s"(tgt) : "s90", "s91");
        }
    }
    __syncthreads();
    unsigned long long w1 = __builtin_amdgcn_s_memtime();
    asm volatile("s_waitcnt lgkmcnt(0)");
#pragma nounroll
    for (int it = 0; it < iters; ++it) {
        unsigned long long t0 = __builtin_amdgcn_s_memtime();
        asm volatile("s_waitcnt lgkmcnt(0)");
        asm volatile(".p2align 6\n\tPREG:\n\t.rept %5\n\ts_branch 1\n\ts_setpc_b64 s[90:91]\n\t.rept %4\n\tv_add3_u32 %0, %0, 1, %1\n\t.endr\n\t.endr" : "+v"(a), "+v"(b), "+v"(c), "+v"(d) : "i"(512 / 8 - 1), "i"(32768 / 512));
        unsigned long long t1 = __builtin_amdgcn_s_memtime();
        asm volatile("s_waitcnt lgkmcnt(0)");
        if (it == 0) dt0 = t1 - t0; else dt1 = t1 - t0;
    }
    if (a + b + c + d == 0x12345) sink[0] = 1;
    if (blockIdx.x == 100) { unsigned long long until = rt() + (w1 - w0) * 10; while (rt() < until) __builtin_amdgcn_s_sleep(8); }
}

extern "C" void kernel_launch(void* const* d_in, const int* in_sizes, int n_in, void* d_out, int out_size, void* d_ws, size_t ws_size, hipStream_t stream) {
    const u16* x = (const u16*)d_in[0]; const u16* wqkv = (const u16*)d_in[1]; const u16* wo = (const u16*)d_in[2];
    const u16* w1 = (const u16*)d_in[4]; const u16* b1 = (const u16*)d_in[5]; const u16* w2 = (const u16*)d_in[6]; const u16* b2 = (const u16*)d_in[7];
    u16* ws = (u16*)d_ws;
    u16* xn = ws; u16* qkv = xn + S * D; u16* h = qkv + S * 1344; u16* o = h + S * F; u16* a = o + S * D; u16* b = a + S * D;
    probe<<<256, 512, 0, stream>>>((unsigned*)((char*)d_ws + (200 << 20)), 2);
    ln_k<<<S, 64, 0, stream>>>(x, xn);
    gemm_k<<<dim3(21, S), 64, 0, stream>>>(xn, wqkv, nullptr, qkv, 1344, D, 0);
    gemm_k<<<dim3(28, S), 64, 0, stream>>>(xn, w1, b1, h, F, D, 1);
    attn_k<<<dim3(S, H), 256, 0, stream>>>(qkv, o);
    gemm_k<<<dim3(7, S), 64, 0, stream>>>(o, wo, nullptr, a, D, D, 0);
    gemm_k<<<dim3(7, S), 64, 0, stream>>>(h, w2, b2, b, D, F, 2);
    fin_k<<<S * D / 256, 256, 0, stream>>>(x, a, b, (u16*)d_out);
}
```

```cpp
#include <hip/hip_runtime.h>
#include <hip/hip_bf16.h>
#include <stdint.h>

typedef unsigned short u16;
static __device__ __forceinline__ float bf2f(u16 v) { return __uint_as_float(((unsigned)v) << 16); }
static __device__ __forceinline__ u16 f2bf(float f) { unsigned u = __float_as_uint(f); u += 0x7fffu + ((u >> 16) & 1u); return (u16)(u >> 16); }
static __device__ __forceinline__ float rbf(float f) { return bf2f(f2bf(f)); }

constexpr int S = 2048, D = 448, F = 1792, H = 16, DH = 28, W = 256;

__global__ void ln_k(const u16* x, u16* xn) {
    int row = blockIdx.x; int lane = threadIdx.x;
    float v[7]; float s = 0;
    for (int i = 0; i < 7; i++) { v[i] = bf2f(x[row * D + lane + 64 * i]); s += v[i]; }
    for (int m = 32; m >= 1; m >>= 1) s += __shfl_xor(s, m);
    float mean = s / D; float q = 0;
    for (int i = 0; i < 7; i++) { float d = v[i] - mean; q += d * d; }
    for (int m = 32; m >= 1; m >>= 1) q += __shfl_xor(q, m);
    float var = rbf(q / D); float mb = rbf(mean);
    float sd = rbf(sqrtf(rbf(var + 1e-5f)));
    for (int i = 0; i < 7; i++) xn[row * D + lane + 64 * i] = f2bf(rbf(v[i] - mb) / sd);
}
__global__ void gemm_k(const u16* A, const u16* B, const u16* bias, u16* C, int N, int K, int mode) {
    int col = blockIdx.x * 64 + threadIdx.x; int row = blockIdx.y;
    if (col >= N) return;
    float acc = 0;
    for (int k = 0; k < K; k++) acc += bf2f(A[row * K + k]) * bf2f(B[k * N + col]);
    acc = rbf(acc);
    if (mode == 1) { acc = rbf(acc + bf2f(bias[col])); acc = acc > 0 ? acc : 0; acc = acc * acc; }
    if (mode == 2) { acc = rbf(acc + bf2f(bias[col])); }
    C[row * N + col] = f2bf(acc);
}
__global__ void attn_k(const u16* qkv, u16* o) {
    int row = blockIdx.x, h = blockIdx.y; int t = threadIdx.x;
    __shared__ float p[256]; __shared__ float red[4];
    int key = row - 255 + t; float s = -1e30f;
    if (key >= 0) { float a = 0; for (int d = 0; d < DH; d++) a += bf2f(qkv[row * 1344 + h * DH + d]) * bf2f(qkv[key * 1344 + 448 + h * DH + d]); s = rbf(rbf(a) * 0.1889822365f); }
    float m = s; for (int k = 32; k >= 1; k >>= 1) m = fmaxf(m, __shfl_xor(m, k));
    if ((t & 63) == 0) red[t >> 6] = m; __syncthreads();
    m = fmaxf(fmaxf(red[0], red[1]), fmaxf(red[2], red[3])); __syncthreads();
    float e = key >= 0 ? __expf(s - m) : 0.f; float l = e; for (int k = 32; k >= 1; k >>= 1) l += __shfl_xor(l, k);
    if ((t & 63) == 0) red[t >> 6] = l; __syncthreads();
    l = red[0] + red[1] + red[2] + red[3];
    p[t] = rbf(e / l); __syncthreads();
    if (t < DH) { float a = 0; for (int j = 0; j < 256; j++) { int kk = row - 255 + j; if (kk >= 0) a += p[j] * bf2f(qkv[kk * 1344 + 896 + h * DH + t]); } o[row * D + h * DH + t] = f2bf(a); }
}
__global__ void fin_k(const u16* x, const u16* a, const u16* b, u16* out) {
    int i = blockIdx.x * 256 + threadIdx.x;
    out[i] = f2bf(rbf(bf2f(x[i]) + bf2f(a[i])) + bf2f(b[i]));
}


typedef unsigned u32x4 __attribute__((ext_vector_type(4)));
__device__ __forceinline__ unsigned long long rt() { return __builtin_amdgcn_s_memrealtime(); }
__global__ void __launch_bounds__(512) probe(unsigned* sink, int iters) {
    unsigned a = threadIdx.x, b = a * 3, c = a * 5, d = a * 7;
    unsigned long long dt0 = 0, dt1 = 0;
    unsigned long long ts = rt();
    if (1) {
        unsigned long long pc; asm volatile("s_getpc_b64 %0" : "=s"(pc));
        const u32x4* p = (const u32x4*)(pc & ~255ull);
        u32x4 acc = {0, 0, 0, 0};
        if (threadIdx.x < 128) acc = p[(blockIdx.x >> 3) * 64 + threadIdx.x];
        a ^= (acc[0] ^ acc[1] ^ acc[2] ^ acc[3]) == 0x1234567;
        __syncthreads();
        while (rt() < ts + 0) __builtin_amdgcn_s_sleep(1);
    }
#pragma nounroll
    for (int it = 0; it < iters; ++it) {
        unsigned long long t0 = __builtin_amdgcn_s_memtime();
        asm volatile("s_waitcnt lgkmcnt(0)");
        asm volatile(".rept %4\n\tv_add3_u32 %0, %0, 1, %1\n\tv_add3_u32 %1, %1, 1, %2\n\tv_add3_u32 %2, %2, 1, %3\n\tv_add3_u32 %3, %3, 1, %0\n\t.endr" : "+v"(a), "+v"(b), "+v"(c), "+v"(d) : "i"(32 * 32));
        unsigned long long t1 = __builtin_amdgcn_s_memtime();
        asm volatile("s_waitcnt lgkmcnt(0)");
        if (it == 0) dt0 = t1 - t0; else dt1 = t1 - t0;
    }
    if (a + b + c + d == 0x12345) sink[0] = 1;
    if (blockIdx.x == 100) { unsigned long long until = rt() + (dt0 - dt1) * 10; while (rt() < until) __builtin_amdgcn_s_sleep(8); }
}

extern "C" void kernel_launch(void* const* d_in, const int* in_sizes, int n_in, void* d_out, int out_size, void* d_ws, size_t ws_size, hipStream_t stream) {
    const u16* x = (const u16*)d_in[0]; const u16* wqkv = (const u16*)d_in[1]; const u16* wo = (const u16*)d_in[2];
    const u16* w1 = (const u16*)d_in[4]; const u16* b1 = (const u16*)d_in[5]; const u16* w2 = (const u16*)d_in[6]; const u16* b2 = (const u16*)d_in[7];
    u16* ws = (u16*)d_ws;
    u16* xn = ws; u16* qkv = xn + S * D; u16* h = qkv + S * 1344; u16* o = h + S * F; u16* a = o + S * D; u16* b = a + S * D;
    probe<<<256, 256, 0, stream>>>((unsigned*)((char*)d_ws + (200 << 20)), 2);
    ln_k<<<S, 64, 0, stream>>>(x, xn);
    gemm_k<<<dim3(21, S), 64, 0, stream>>>(xn, wqkv, nullptr, qkv, 1344, D, 0);
    gemm_k<<<dim3(28, S), 64, 0, stream>>>(xn, w1, b1, h, F, D, 1);
    attn_k<<<dim3(S, H), 256, 0, stream>>>(qkv, o);
    gemm_k<<<dim3(7, S), 64, 0, stream>>>(o, wo, nullptr, a, D, D, 0);
    gemm_k<<<dim3(7, S), 64, 0, stream>>>(h, w2, b2, b, D, F, 2);
    fin_k<<<S * D / 256, 256, 0, stream>>>(x, a, b, (u16*)d_out);
}
```

```cpp
#include <hip/hip_runtime.h>
#include <stdint.h>

typedef unsigned long long u64;
typedef short bf16x8 __attribute__((ext_vector_type(8)));
typedef short s16x4 __attribute__((ext_vector_type(4)));
typedef __bf16 b16x2 __attribute__((ext_vector_type(2)));
typedef float f32x4 __attribute__((ext_vector_type(4)));
typedef float f32x2 __attribute__((ext_vector_type(2)));
typedef unsigned u32x4 __attribute__((ext_vector_type(4)));
typedef unsigned u32x2 __attribute__((ext_vector_type(2)));
typedef __amdgpu_buffer_rsrc_t rsrc_t;
#define LAS __attribute__((address_space(3)))
#define GAS __attribute__((address_space(1)))
#define DI __device__ __forceinline__
#define RLX_AGENT __ATOMIC_RELAXED, __HIP_MEMORY_SCOPE_AGENT

constexpr unsigned WS_E = 0;
constexpr unsigned WS_FQ = 4096;
constexpr unsigned WS_FH = 32768;
constexpr unsigned WS_FO = 65536;
constexpr unsigned WS_GS = 98304;
constexpr unsigned WS_Q = 131072;
constexpr unsigned WS_K = WS_Q + 16 * 64 * 1792;
constexpr unsigned WS_V = WS_K + 16 * 64 * 1792;
constexpr unsigned WS_H = WS_V + 16 * 64 * 2048;
constexpr unsigned WS_O = WS_H + 128 * 56 * 1024;
constexpr unsigned WS_END = WS_O + 128 * 16 * 1024;

constexpr int UNIT = 8192;
constexpr int phys(int piece) { return (((piece % 19) + 3) % 19) * UNIT; }
constexpr int L_REG = 12 * UNIT;
constexpr int L_Q = L_REG, L_K = L_REG + UNIT, L_V = L_REG + 4 * UNIT;
constexpr int L_TB = 19 * UNIT;
constexpr int L_CS = L_TB, L_B1 = L_TB + 1024, L_B2 = L_B1 + 768, L_JUNK = L_TB + 2048;
constexpr int L_RED = 4 * UNIT, RED_RS = 272;
constexpr int LDS_BYTES = 163840;

#define AFLAG_AT 7
#define OFLAG_AT 2
constexpr unsigned SPIN_MAX = 400000;

#define MFMA(a, b, c) __builtin_amdgcn_mfma_f32_16x16x32_bf16((a), (b), (c), 0, 0, 0)
#define WAITBAR(N) asm volatile("s_waitcnt vmcnt(%0) lgkmcnt(0)\n\ts_barrier" ::"i"(N) : "memory")
#define BAR() asm volatile("s_waitcnt lgkmcnt(0)\n\ts_barrier" ::: "memory")
#define WAITV(N) asm volatile("s_waitcnt vmcnt(%0)" ::"i"(N) : "memory")
#define SB() __builtin_amdgcn_sched_barrier(0)
#define PINV(x) asm volatile("" : "+v"(x))
#define PINS(x) asm volatile("" : "+s"(x))
#define ZERO4 ((f32x4){0.f, 0.f, 0.f, 0.f})

DI u32x4 mkrsrc(const char* p, unsigned bytes) { const u64 a = (u64)p; return (u32x4){(unsigned)a, (unsigned)(a >> 32), bytes, 0x20000u}; }
template <int DST> DI void dma16(unsigned voff, u32x4 r, unsigned soff, unsigned wk) {
    asm volatile("s_add_u32 m0, %3, %4\n\ts_nop 0\n\tbuffer_load_dwordx4 %0, %1, %2 offen lds" ::"v"(voff), "s"(r), "s"(soff), "s"(wk), "i"(DST) : "memory", "scc");
}
template <int DST, int SOFF> DI void dma16i(unsigned voff, u32x4 r, unsigned wk) {
    unsigned t;
    asm volatile("s_add_u32 m0, %3, %4\n\ts_mov_b32 %0, %5\n\tbuffer_load_dwordx4 %1, %2, %0 offen lds" : "=&s"(t) : "v"(voff), "s"(r), "s"(wk), "i"(DST), "i"(SOFF) : "memory", "scc");
}
template <int DST> DI void dma16c(unsigned voff, u32x4 r, unsigned wk) {
    asm volatile("s_add_u32 m0, %2, %3\n\ts_nop 0\n\tbuffer_load_dwordx4 %0, %1, 0 offen sc1 lds" ::"v"(voff), "s"(r), "s"(wk), "i"(DST) : "memory", "scc");
}
DI void gdma4(unsigned voff, const char* sbase, unsigned ldsdst) {
    asm volatile("s_mov_b32 m0, %2\n\ts_nop 0\n\tglobal_load_lds_dword %0, %1" ::"v"(voff), "s"(sbase), "s"(ldsdst) : "memory");
}
DI bf16x8 lds128(unsigned a) { return *(const LAS bf16x8*)(size_t)a; }
DI f32x4 ldsf4(unsigned a) { return *(const LAS f32x4*)(size_t)a; }
DI float ldsf(unsigned a) { return *(const LAS float*)(size_t)a; }
DI s16x4 ldstr(unsigned a) { return __builtin_amdgcn_ds_read_tr16_b64_v4i16((LAS s16x4*)(size_t)a); }
DI unsigned pk2(float a, float b) { f32x2 p = {a, b}; return __builtin_bit_cast(unsigned, __builtin_convertvector(p, b16x2)); }
DI float rbf(float a) { return __uint_as_float(pk2(a, 0.f) << 16); }
DI float bflo(unsigned u) { return __uint_as_float(u << 16); }
DI float bfhi(unsigned u) { return __uint_as_float(u & 0xffff0000u); }
DI bf16x8 asf(u32x4 v) { return __builtin_bit_cast(bf16x8, v); }
DI float redmax4(float v) {
    auto a = __builtin_amdgcn_permlane16_swap(__float_as_uint(v), __float_as_uint(v), false, false); v = __builtin_fmaxf(__uint_as_float(a[0]), __uint_as_float(a[1]));
    auto b = __builtin_amdgcn_permlane32_swap(__float_as_uint(v), __float_as_uint(v), false, false); return __builtin_fmaxf(__uint_as_float(b[0]), __uint_as_float(b[1])); }
template <int CTRL> DI float dppadd(float v) { return v + __uint_as_float(__builtin_amdgcn_update_dpp(0, __float_as_uint(v), CTRL, 0xf, 0xf, true)); }
DI float wavesum(float v) {
    v = dppadd<0xB1>(v); v = dppadd<0x4E>(v); v = dppadd<0x141>(v); v = dppadd<0x140>(v);
    auto a = __builtin_amdgcn_permlane16_swap(__float_as_uint(v), __float_as_uint(v), false, false); v = __uint_as_float(a[0]) + __uint_as_float(a[1]);
    auto b = __builtin_amdgcn_permlane32_swap(__float_as_uint(v), __float_as_uint(v), false, false); return __uint_as_float(b[0]) + __uint_as_float(b[1]); }
DI void put_flag(rsrc_t rs, unsigned off, u32x2 v) { __builtin_amdgcn_raw_buffer_store_b64(v, rs, off, 0, 16); }
DI u32x2 get_flag(rsrc_t rs, unsigned off) { return __builtin_amdgcn_raw_buffer_load_b64(rs, off, 0, 16); }
DI bool all_set(u32x2 f, u32x2 ep) { return __builtin_amdgcn_ballot_w64(((f[0] ^ ep[0]) | (f[1] ^ ep[1])) != 0) == 0; }
DI void spin_flags(rsrc_t rs, unsigned off, u32x2 ep) {
#pragma nounroll
    for (unsigned it = 0; it < SPIN_MAX; ++it) {
        asm volatile("" ::: "memory");
        if (all_set(get_flag(rs, off), ep)) break;
        __builtin_amdgcn_s_sleep(1);
    }
    asm volatile("" ::: "memory");
}
DI unsigned wrap7(unsigned v) { return v >= 7 ? v - 7 : v; }
constexpr int blk_at(int j) { return j < 4 ? 5 + 2 * j : (j < 9 ? j - 4 : (j < 13 ? 2 * j - 12 : 13)); }
constexpr int n_r(int i) { return (i >= 0 && i <= 7) ? 2 : 0; }
constexpr int n_h(int i) { return (i >= 0 && i <= 5) ? 2 : 0; }
constexpr int n_wo(int i) { return (i >= 6 && i <= 8) ? 2 : (i == 9 ? 1 : 0); }
constexpr int n_f(int i) { return i == OFLAG_AT ? 1 : 0; }
constexpr int cnt_w(int j) { if (j == 12) return 0; int c = n_h(j - 5) + n_wo(j - 5) + n_f(j - 5); for (int i = j - 4; i < j; ++i) c += n_r(i) + n_h(i) + n_wo(i) + n_f(i); return c; }
constexpr int cnt_o() { int c = 6; for (int i = 0; i <= OFLAG_AT; ++i) c += n_r(i) + n_h(i) + n_wo(i); return c; }

#define STAMP(i) do { } while (0)
#define DIAGPAR
#define DIAGARG

struct HalfFrag { s16x4 lo, hi; };
DI bf16x8 join(const HalfFrag& h) { return __builtin_shufflevector(h.lo, h.hi, 0, 1, 2, 3, 4, 5, 6, 7); }
DI void rd_tr(HalfFrag& h, unsigned base, unsigned c, unsigned d) {
    if (c + d < 65536) { h.lo = ldstr(base + c); h.hi = ldstr(base + c + d); }
    else { unsigned a = base + c; PINV(a); h.lo = ldstr(a); h.hi = ldstr(a + d); }
}

struct Gran { u32x2 f[2]; u32x4 q[2][2]; };
struct P1 {
    unsigned xa[2], wa[2][2];
    u32x4 rx, rq, r1, rw;
    unsigned xv0, xv1, v0, v1, v2;
    unsigned m1, wk, c0;
    rsrc_t rs; unsigned gf, gq, gs, tag, lane;
    u32x4 srow; float sv[8], sm, sq;
};
template <int N> DI void piece(const P1& P) {
    if constexpr (N < 28) {
        const unsigned kc = wrap7(P.c0 + (N >> 2)) * 128;
        if constexpr ((N & 3) == 0) dma16<phys(N)>(P.xv0, P.rx, kc, P.wk);
        else if constexpr ((N & 3) == 1) dma16<phys(N)>(P.xv1, P.rx, kc, P.wk);
        else if constexpr ((N & 3) == 2) dma16<phys(N)>(P.v0, P.rq, kc * 1344, P.wk);
        else dma16<phys(N)>(P.v1, P.r1, kc * P.m1, P.wk);
    } else if constexpr (N < 35) {
        dma16<phys(N)>(P.v2, P.rw, wrap7(P.c0 + (N - 28)) * (128 * 1792), P.wk);
    }
}
template <int S> DI void rd_ax(bf16x8 (&xf)[14][2], int tt, const P1& P) { if constexpr (S < 14) xf[S][tt] = lds128(P.xa[S & 1] + phys(4 * (S >> 1)) + tt * 2048); }
template <int S> DI void rd_aw(HalfFrag (&wf)[2][2][2], int b, int nt, const P1& P) { if constexpr (S < 14) rd_tr(wf[S & 1][b][nt], P.wa[S & 1][nt], phys(4 * (S >> 1) + 2 + b), 512); }
constexpr int a_first(int s) { return s == 0 ? 8 : (s == 1 ? 12 : (s == 2 ? 16 : 14 + 2 * s)); }
constexpr int a_count(int s) { return s <= 2 ? 4 : 2; }
DI void ld_gran(Gran& R, const P1& P) {
#pragma unroll
    for (int tt = 0; tt < 2; ++tt) {
        R.f[tt] = __builtin_amdgcn_raw_buffer_load_b64(P.rs, P.gf + tt * 128, 0, 16);
        R.q[tt][0] = __builtin_amdgcn_raw_buffer_load_b128(P.rs, P.gq + tt * 128, 0, 16); R.q[tt][1] = __builtin_amdgcn_raw_buffer_load_b128(P.rs, P.gq + tt * 128 + 16, 0, 16);
    }
}
template <int S> DI void stats_stage(P1& P) {
    if constexpr (S == 0) {
#pragma unroll
        for (int j = 0; j < 4; ++j) { const unsigned u = P.srow[j]; P.sv[2 * j] = bflo(u); P.sv[2 * j + 1] = bfhi(u); }
        P.sm = ((P.sv[0] + P.sv[1]) + (P.sv[2] + P.sv[3])) + ((P.sv[4] + P.sv[5]) + (P.sv[6] + P.sv[7])); PINV(P.sm);
    } else if constexpr (S == 1) { P.sm = wavesum(P.sm) * (1.f / 448.f); PINV(P.sm);
    } else if constexpr (S == 2) {
        float q0 = 0.f, q1 = 0.f;
#pragma unroll
        for (int j = 0; j < 4; ++j) { const float d0 = P.sv[j] - P.sm, d1 = P.sv[j + 4] - P.sm; q0 = __builtin_fmaf(d0, d0, q0); q1 = __builtin_fmaf(d1, d1, q1); }
        P.sq = P.lane < 56 ? q0 + q1 : 0.f; PINV(P.sq);
    } else if constexpr (S == 3) { P.sq = wavesum(P.sq) * (1.f / 448.f); PINV(P.sq);
    } else if constexpr (S == 4) {
        const unsigned sdm = pk2(__builtin_amdgcn_sqrtf(rbf(rbf(P.sq) + 1e-5f)), P.sm);
        put_flag(P.rs, P.gs, (u32x2){sdm, P.tag});
    }
}
template <bool B1Q, int S>
DI void step_a(bf16x8 (&xf)[14][2], HalfFrag (&wf)[2][2][2], f32x4 (&acc)[2][2][2], f32x4 (&cs)[2], bf16x8 ones, P1& P, Gran& R) {
    if constexpr ((S & 1) && S < 13) {
        constexpr int c = (S + 1) >> 1;
        if constexpr (c == 1) WAITBAR(4); else if constexpr (c == 2) WAITBAR(8); else if constexpr (c < 6) WAITBAR(9); else WAITBAR(7);
    }
    constexpr int f = a_first(S), n = a_count(S);
#define MA(b, nt, tt) do { acc[b][nt][tt] = (b == 0 || B1Q) ? MFMA(xf[S][tt], join(wf[S & 1][b][nt]), acc[b][nt][tt]) : MFMA(join(wf[S & 1][b][nt]), xf[S][tt], acc[b][nt][tt]); } while (0)
    MA(0, 0, 0); rd_ax<S + 1>(xf, 0, P); SB();
    MA(0, 0, 1); rd_aw<S + 1>(wf, 0, 0, P); SB();
    MA(0, 1, 0); piece<f>(P); SB();
    MA(0, 1, 1); rd_ax<S + 1>(xf, 1, P); SB();
    if constexpr (n > 2) { piece<f + 2>(P); SB(); }
    MA(1, 0, 0); rd_aw<S + 1>(wf, 0, 1, P); SB();
    MA(1, 0, 1); piece<f + 1>(P); SB();
    MA(1, 1, 0); rd_aw<S + 1>(wf, 1, 0, P); SB();
    MA(1, 1, 1); rd_aw<S + 1>(wf, 1, 1, P); SB();
#undef MA
    if constexpr ((S & 1) == 0) { cs[0] = MFMA(ones, join(wf[0][0][0]), cs[0]); cs[1] = MFMA(ones, join(wf[0][1][0]), cs[1]); SB(); }
    if constexpr (n > 2) { piece<f + 3>(P); SB(); }
    if constexpr (S <= 4) { stats_stage<S>(P); SB(); }
    if constexpr (S == 11) { ld_gran(R, P); SB(); }
    if constexpr (S < 13) step_a<B1Q, S + 1>(xf, wf, acc, cs, ones, P, R);
}
template <bool B1Q>
DI void pass_a(bf16x8 (&xf)[14][2], f32x4 (&acc)[2][2][2], f32x4 (&cs)[2], bf16x8 ones, P1& P, Gran& R DIAGPAR) {
    HalfFrag wf[2][2][2];
    STAMP(13);
    rd_ax<0>(xf, 0, P); rd_aw<0>(wf, 0, 0, P); rd_ax<0>(xf, 1, P); rd_aw<0>(wf, 0, 1, P); rd_aw<0>(wf, 1, 0, P); rd_aw<0>(wf, 1, 1, P); SB();
    step_a<B1Q, 0>(xf, wf, acc, cs, ones, P, R);
}
DI unsigned qkv_dest(unsigned n, unsigned tb, unsigned g) {
    const unsigned hs = (n * 2341u) >> 16, d = n - hs * 28u;
    const unsigned isv = hs >> 5, pl = 448 + 64 * isv;
    unsigned o = WS_Q - isv * (32 * 64 * 256) + (hs * 256 + tb * 4 + g) * pl + d * 16 + ((d >> 3) & 1 & ~isv);
    PINV(o); return o;
}
DI void epi_qkv(const f32x4 (&acc)[2][2], const f32x4 (&mr)[2], const f32x4 (&rq)[2], const float (&cs)[2], const unsigned (&q)[2], rsrc_t rs) {
#pragma unroll
    for (int nt = 0; nt < 2; ++nt) {
        unsigned w[2][2];
#pragma unroll
        for (int tt = 0; tt < 2; ++tt) {
            const f32x4 v = acc[nt][tt] * rq[tt] - mr[tt] * cs[nt];
            w[tt][0] = pk2(v[0], v[1]); w[tt][1] = pk2(v[2], v[3]);
        }
        const bool sw = q[nt] & 1;
        const u32x4 o = {sw ? w[1][0] : w[0][0], sw ? w[1][1] : w[0][1], sw ? w[0][0] : w[1][0], sw ? w[0][1] : w[1][1]};
        __builtin_amdgcn_raw_buffer_store_b128(o, rs, q[nt] & ~15u, 0, 16);
    }
}
DI void epi_ffn(const f32x4 (&acc)[2][2], u32x4 (&o)[2], const unsigned (&ta)[2], int slot, int b1i, bool s1, const float (&mr)[2], const float (&rstd)[2]) {
    f32x4 cs[2], bb[2];
#pragma unroll
    for (int nt = 0; nt < 2; ++nt) {
        cs[nt] = ldsf4(L_CS + (slot & 1) * 512 + ta[nt] * 2) + ldsf4(L_CS + (slot & 1) * 512 + 256 + ta[nt] * 2);
        const u32x2 b = *(const LAS u32x2*)(size_t)(L_B1 + b1i * 256 + ta[nt]);
        bb[nt] = (f32x4){bflo(b[0]), bfhi(b[0]), bflo(b[1]), bfhi(b[1])};
    }
#pragma unroll
    for (int tt = 0; tt < 2; ++tt) {
        unsigned w[2][2];
#pragma unroll
        for (int nt = 0; nt < 2; ++nt) {
            f32x4 u = acc[nt][tt] * rstd[tt] + (bb[nt] - mr[tt] * cs[nt]);
            u = __builtin_elementwise_max(u, ZERO4); u = u * u;
            w[nt][0] = pk2(u[0], u[1]); w[nt][1] = pk2(u[2], u[3]);
        }
        o[tt] = s1 ? (u32x4){w[1][0], w[1][1], w[0][0], w[0][1]} : (u32x4){w[0][0], w[0][1], w[1][0], w[1][1]};
    }
}
template <int S> DI void rd_b(HalfFrag (&wf)[3][2], int nt, const unsigned (&wa)[2][2]) { if constexpr (S < 14) rd_tr(wf[S % 3][nt], wa[S & 1][nt], (S >> 1) * UNIT, 512); }
struct PB { u32x4 r; rsrc_t rs; u32x2 ep; unsigned v, wk, f0, f1, c0; };
template <int MODE, int S>
DI void step_b(const bf16x8 (&xf)[14][2], HalfFrag (&wf)[3][2], f32x4 (&ac)[2][2], f32x4& cs, bf16x8 ones, const unsigned (&wa)[2][2], const PB& B) {
    ac[0][0] = MFMA(join(wf[S % 3][0]), xf[S][0], ac[0][0]); rd_b<S + 2>(wf, 0, wa); SB();
    ac[0][1] = MFMA(join(wf[S % 3][0]), xf[S][1], ac[0][1]); SB();
    if constexpr (MODE == 0 && S < 12) { dma16i<S * UNIT, (blk_at(S >> 1) + 14 * (S & 1)) * (64 * 896)>(B.v, B.r, B.wk); SB(); }
    if constexpr (MODE == 1 && S < 7) { dma16<S * UNIT>(B.v, B.r, wrap7(B.c0 + S) * (128 * 1792), B.wk); SB(); }
    ac[1][0] = MFMA(join(wf[S % 3][1]), xf[S][0], ac[1][0]); rd_b<S + 2>(wf, 1, wa); SB();
    ac[1][1] = MFMA(join(wf[S % 3][1]), xf[S][1], ac[1][1]);
    if constexpr ((S & 1) == 0) cs = MFMA(ones, join(wf[S % 3][0]), cs);
    SB();
    if constexpr (S == AFLAG_AT) {
        WAITV(MODE == 0 ? (S + 1 < 12 ? S + 1 : 12) : (MODE == 1 ? (S + 1 < 7 ? S + 1 : 7) : 0));
        put_flag(B.rs, B.f0, B.ep);
        if constexpr (MODE < 2) put_flag(B.rs, B.f1, B.ep);
        SB();
    }
    if constexpr (S < 13) step_b<MODE, S + 1>(xf, wf, ac, cs, ones, wa, B);
}
template <int MODE>
DI void pass_b(const bf16x8 (&xf)[14][2], u32x4 (&o)[2], bf16x8 ones, const P1& P, const PB& B, unsigned csw, const unsigned (&ta)[2], bool s1, const float (&mr)[2], const float (&rstd)[2]) {
    unsigned wa[2][2];
#pragma unroll
    for (int e = 0; e < 2; ++e)
#pragma unroll
        for (int nt = 0; nt < 2; ++nt) { wa[e][nt] = P.wa[e][nt] + (MODE < 2 ? L_REG : 0); PINV(wa[e][nt]); }
    f32x4 ac[2][2] = {{ZERO4, ZERO4}, {ZERO4, ZERO4}}, c2 = ZERO4;
    HalfFrag wf[3][2];
    rd_b<0>(wf, 0, wa); rd_b<0>(wf, 1, wa); rd_b<1>(wf, 0, wa); rd_b<1>(wf, 1, wa); SB();
    step_b<MODE, 0>(xf, wf, ac, c2, ones, wa, B);
    *(LAS float*)(size_t)(csw + (MODE == 2 ? 512 : 0)) = c2[0];
    BAR();
    epi_ffn(ac, o, ta, MODE == 2 ? 1 : 0, MODE == 2 ? 0 : 1, s1, mr, rstd);
}

template <int J> DI void rd_c(HalfFrag (&wf)[2][2][4], int e, int nt, const unsigned (&wb)[4]) { rd_tr(wf[J & 1][e][nt], wb[nt], ((2 * J) % 12) * UNIT + e * 4096, 2048); }
struct PC { u32x4 rw2, rwo; rsrc_t rs; u32x2 ep; unsigned v, wk, hbase, fo; };
template <int I> DI void ld_h(u32x4 (&hf)[28], const PC& C) { hf[I] = __builtin_amdgcn_raw_buffer_load_b128(C.rs, C.hbase + (I & 1) * 1024, blk_at(I >> 1) * 2048, 16); }
template <int J>
DI void step_c(u32x4 (&hf)[28], HalfFrag (&wf)[2][2][4], f32x4 (&ac)[4], const unsigned (&wb)[4], const PC& C) {
    if constexpr (J >= 5) WAITBAR(cnt_w(J)); else BAR();
#define MC(e, nt) do { ac[nt] = MFMA(join(wf[J & 1][e][nt]), asf(hf[2 * J + e]), ac[nt]); rd_c<J + 1>(wf, e, nt, wb); SB(); } while (0)
    MC(0, 0); MC(0, 1);
    if constexpr (J <= 7) { dma16i<((2 * J) % 12) * UNIT, blk_at(J + 6) * (64 * 896)>(C.v, C.rw2, C.wk); SB(); }
    MC(0, 2); MC(0, 3);
    if constexpr (J <= 7) { dma16i<((2 * J + 1) % 12) * UNIT, (blk_at(J + 6) + 14) * (64 * 896)>(C.v, C.rw2, C.wk); SB(); }
    MC(1, 0); MC(1, 1);
    if constexpr (J <= 5) { ld_h<2 * (J + 7)>(hf, C); SB(); }
    if constexpr (J >= 6 && J <= 9) { dma16i<L_REG + (2 * (J - 6)) * UNIT, (2 * (J - 6)) * (64 * 896)>(C.v, C.rwo, C.wk); SB(); }
    MC(1, 2); MC(1, 3);
#undef MC
    if constexpr (J <= 5) { ld_h<2 * (J + 7) + 1>(hf, C); SB(); }
    if constexpr (J >= 6 && J < 9) { dma16i<L_REG + (2 * (J - 6) + 1) * UNIT, (2 * (J - 6) + 1) * (64 * 896)>(C.v, C.rwo, C.wk); SB(); }
    if constexpr (J == OFLAG_AT) { WAITV(cnt_o()); put_flag(C.rs, C.fo, C.ep); SB(); }
    if constexpr (J < 12) step_c<J + 1>(hf, wf, ac, wb, C);
}

constexpr unsigned OOB = 0x80000000u;

__global__ void __launch_bounds__(512, 2) fused_k(const char* __restrict__ x, const char* __restrict__ wqkv, const char* __restrict__ wo, const char* __restrict__ w1,
                                                  const char* __restrict__ b1, const char* __restrict__ w2, const char* __restrict__ b2, char* __restrict__ out, char* ws) {
    STAMP(0);
    const unsigned tid = threadIdx.x, lane = tid & 63, wv = __builtin_amdgcn_readfirstlane(tid >> 6);
    const unsigned bid = blockIdx.x, xcd = bid & 7, idx = bid >> 3;
    {
        u64 pc; unsigned len;
        asm volatile("s_getpc_b64 %0\n.Lfk_begin:\n\ts_mov_b32 %1, .Lfk_end-.Lfk_begin-256" : "=s"(pc), "=s"(len));
        gdma4(lane * 4, (const char*)(pc + min(idx * 1024 + wv * 256, len)), L_JUNK + wv * 256);
    }
    const unsigned rt = (xcd & 1) * 8 + (idx & 7), G = xcd >> 1, slot = G * 4 + (idx >> 3);
    const bool two = slot < 5;
    const unsigned qb0 = two ? 2 * slot : slot + 5;
    const unsigned blk1 = two ? 2 * slot + 1 : 2 * slot - 5;
    const unsigned fb2 = two ? slot : 2 * slot - 4;
    const unsigned drow = tid >> 3, dc = lane & 7;
    P1 P;
    P.wk = wv * 1024; P.c0 = (rt + slot) - 7 * (((rt + slot) * 37) >> 8); P.lane = lane;
    P.xv0 = (rt * 128 + drow) * 896 + ((dc ^ ((drow >> 1) & 7)) << 4); P.xv1 = P.xv0 + 64 * 896;
    const unsigned wsw1 = (dc ^ ((drow & 2) | ((drow >> 1) & 4))) << 4;
    P.v0 = drow * 2688 + wsw1 + qb0 * 128; P.v2 = drow * 3584 + wsw1;
    P.m1 = two ? 1344 : 1792; P.v1 = drow * 2 * P.m1 + wsw1 + blk1 * 128;
    const unsigned srow_off = (rt * 128 + slot * 8 + wv) * 896 + lane * 16 + (lane < 56 ? 0 : OOB);
    asm volatile("" : "+s"(x), "+s"(wqkv), "+s"(wo), "+s"(w1), "+s"(b1), "+s"(w2), "+s"(b2), "+s"(out), "+s"(ws));
    P.rx = mkrsrc(x, 2048 * 896); P.rq = mkrsrc(wqkv, 448 * 2688); P.rw = mkrsrc(w1, 448 * 3584); P.r1 = two ? P.rq : P.rw;
    const u64 e0 = *(const __attribute__((address_space(4))) u64*)(ws + WS_E);
    asm volatile("buffer_load_dwordx4 %0, %1, %2, 0 offen" : "=v"(P.srow) : "v"(srow_off), "s"(P.rx) : "memory");
    piece<0>(P); piece<1>(P); piece<2>(P); piece<3>(P);
    const bool spare = idx - (G == 0 ? 24u : 28u) < 4u;
    const bool extra = spare && G < 2;
    const unsigned p3 = spare ? 0 : (idx >= 28 ? idx - 4 : idx);
    const unsigned p37 = (p3 * 37) >> 8, rt64 = 4 * xcd + p37, cb = p3 - 7 * p37;
    {
        const unsigned w3 = wv & 3, sel = 27u | (fb2 << 8) | ((blk1 & 31) << 16) | (cb << 24);
        gdma4((lane & 31) * 4 + ((sel >> (8 * w3)) & 31) * 128, w3 == 3 ? b2 : b1, L_B1 + w3 * 256 + (wv >> 2) * 4096);
    }
    piece<4>(P); piece<5>(P); piece<6>(P); piece<7>(P);
    const unsigned vx27 = P.v2 + 27 * 128;
    P.v2 += fb2 * 128;
    STAMP(12);
    const unsigned r16 = lane & 15, g = lane >> 4, q4 = r16 >> 2, p4 = lane & 3;
    const unsigned slab = wv & 3, ch = wv >> 2, s1 = slab >> 1;
    const u64 ep64 = e0 * 0xD1342543DE82EF95ull + 0x9E3779B97F4A7C15ull;
    const u32x2 ep = {(unsigned)ep64, (unsigned)(ep64 >> 32)};
    const rsrc_t rs = __builtin_amdgcn_make_buffer_rsrc(ws, 0, (int)WS_END, 0x00020000);
    const unsigned l0 = lane == 0 ? 0 : OOB;
    {
        const unsigned fl = r16 >> 1, key = ((q4 >> 1) & 1) | ((g & 1) << 1);
#pragma unroll
        for (unsigned e = 0; e < 2; ++e) {
            const unsigned ee = e ^ ch ^ (slab & 1);
            P.xa[e] = (slab >> 1) * UNIT + ((slab & 1) * 32 + r16) * 128 + (((4 * ee + g) ^ fl) << 4);
#pragma unroll
            for (unsigned nt = 0; nt < 2; ++nt) P.wa[e][nt] = ee * 4096 + (8 * g + q4) * 128 + (((2 * ch + (nt ^ s1)) ^ key) << 5) + p4 * 8;
        }
    }
    const unsigned tb = 4 * rt + slab;
    P.rs = rs; P.tag = ep[1]; P.gf = WS_GS + (tb * 32 + r16) * 8; P.gq = WS_GS + (tb * 32 + 4 * g) * 8; P.gs = WS_GS + (rt * 128 + slot * 8 + wv) * 8 + l0;
    unsigned qd[2][2], ta[2];
#pragma unroll
    for (unsigned nt = 0; nt < 2; ++nt) {
        const unsigned nl = 32 * ch + 16 * (nt ^ s1);
        qd[0][nt] = qkv_dest(64 * qb0 + nl + r16, tb, g); qd[1][nt] = qkv_dest(64 * (two ? blk1 : qb0) + nl + r16, tb, g);
        ta[nt] = (nl + 4 * g) * 2;
    }
    u32x4 ones4 = {0x3f803f80u, 0x3f803f80u, 0x3f803f80u, 0x3f803f80u}; PINV(ones4);
    const bf16x8 ones = asf(ones4);
    bf16x8 xf[14][2];
    f32x4 acc[2][2][2], cs[2] = {ZERO4, ZERO4};
#pragma unroll
    for (int b = 0; b < 2; ++b)
#pragma unroll
        for (int nt = 0; nt < 2; ++nt)
#pragma unroll
            for (int tt = 0; tt < 2; ++tt) acc[b][nt][tt] = ZERO4;
    Gran R;
    asm volatile("s_waitcnt vmcnt(5) lgkmcnt(0)\n\ts_barrier" : "+v"(P.srow)::"memory");
    if (two) pass_a<true>(xf, acc, cs, ones, P, R DIAGARG);
    else pass_a<false>(xf, acc, cs, ones, P, R DIAGARG);
    STAMP(1);
    const unsigned csw = L_CS + (slab & 1) * 256 + (32 * ch + 16 * s1 + r16) * 4;
    *(LAS float*)(size_t)(csw) = cs[0][0];
    *(LAS float*)(size_t)(csw + 512) = cs[1][0];
#pragma nounroll
    for (unsigned it = 0; it < SPIN_MAX; ++it) {
        unsigned bad = (R.f[0][1] ^ P.tag) | (R.f[1][1] ^ P.tag);
#pragma unroll
        for (int i = 0; i < 4; ++i) bad |= (R.q[i >> 1][i & 1][1] ^ P.tag) | (R.q[i >> 1][i & 1][3] ^ P.tag);
        if (__builtin_amdgcn_ballot_w64(bad != 0) == 0) break;
        __builtin_amdgcn_s_sleep(1);
        asm volatile("" ::: "memory");
        ld_gran(R, P);
    }
    float mr[2], rstd[2], csq[2][2];
    f32x4 mq[2], rq[2];
#pragma unroll
    for (int tt = 0; tt < 2; ++tt) {
        rstd[tt] = __builtin_amdgcn_rcpf(bflo(R.f[tt][0])); mr[tt] = bfhi(R.f[tt][0]) * rstd[tt];
#pragma unroll
        for (int j = 0; j < 4; ++j) { const unsigned u = R.q[tt][j >> 1][2 * (j & 1)]; rq[tt][j] = __builtin_amdgcn_rcpf(bflo(u)); mq[tt][j] = bfhi(u); }
        mq[tt] *= rq[tt]; PINV(mq[tt]);
    }
    BAR();
#pragma unroll
    for (int b = 0; b < 2; ++b)
#pragma unroll
        for (int nt = 0; nt < 2; ++nt) { const unsigned a = (csw & ~(256u | 64u)) + b * 512 + (nt ^ s1) * 64; csq[b][nt] = ldsf(a) + ldsf(a + 256); }
    const unsigned hst = WS_H + (2 * tb * 56 + ch) * 1024 + lane * 16;
    epi_qkv(acc[0], mq, rq, csq[0], qd[0], rs);
    if (two) epi_qkv(acc[1], mq, rq, csq[1], qd[1], rs);
    else {
        u32x4 o[2]; epi_ffn(acc[1], o, ta, 1, 2, s1, mr, rstd);
#pragma unroll
        for (int tt = 0; tt < 2; ++tt) __builtin_amdgcn_raw_buffer_store_b128(o[tt], rs, hst + blk1 * 2048 + tt * 56 * 1024, 0, 16);
    }
    STAMP(2);
    const unsigned wv3 = drow * 896 + ((dc ^ (drow & 6)) << 4) + cb * 128;
    const u32x4 rw2 = mkrsrc(w2, 1792 * 896);
    const unsigned fhw = WS_FH + (tb * 56 + ch) * 8 + l0;
    PB B; B.rs = rs; B.ep = ep; B.wk = P.wk; B.c0 = P.c0;
    B.f0 = WS_FQ + ((2 * qb0 + ch) * 64 + tb) * 8 + l0;
    B.f1 = two ? B.f0 + 128 * 8 : fhw + blk1 * 16;
    const unsigned hd = 2 * xcd + (idx & 1), art = idx >> 1;
    u32x4 ob[2]; unsigned fb = fb2;
    WAITBAR(4);
    STAMP(15);
    if (!extra) { B.r = rw2; B.v = wv3; pass_b<0>(xf, ob, ones, P, B, csw, ta, s1, mr, rstd); }
    else {
        B.r = P.rw; B.v = vx27; pass_b<1>(xf, ob, ones, P, B, csw, ta, s1, mr, rstd);
#pragma unroll
        for (int tt = 0; tt < 2; ++tt) __builtin_amdgcn_raw_buffer_store_b128(ob[tt], rs, hst + fb2 * 2048 + tt * 56 * 1024, 0, 16);
        WAITBAR(AFLAG_AT >= 6 ? 4 : 2);
        B.f0 = fhw + fb2 * 16; fb = 27;
        pass_b<2>(xf, ob, ones, P, B, csw, ta, s1, mr, rstd);
    }
    STAMP(3);
    unsigned tid2 = threadIdx.x; PINV(tid2);
    const unsigned lane2 = tid2 & 63, r16b = lane2 & 15, gb = lane2 >> 4, q4b = r16b >> 2, p4b = lane2 & 3;
    if (wv == 0) {
        const unsigned sel = lane2 >= 28, m = min(lane2 - 28 * sel, 27u);
        const unsigned sec = (m >= 4) + (m >= 16);
        const int t = (int)(4 * art + m) - 12 * (int)sec;
        spin_flags(rs, WS_FQ + (((448 * sec + 28 * hd + 27 * sel) >> 5) * 64 + (t < 0 ? 4 * art : t)) * 8, ep);
    }
#pragma unroll
    for (int tt = 0; tt < 2; ++tt) __builtin_amdgcn_raw_buffer_store_b128(ob[tt], rs, hst + fb * 2048 + tt * 56 * 1024, 0, 16);
    if (tid2 < 12) *(LAS u32x4*)(size_t)(L_V + tid2 * 2048 + 28 * 16) = ones4;
    const unsigned tt3 = wv & 3, kh = wv >> 2;
    const unsigned fhoff = WS_FH + ((2 * rt64 + (tt3 >> 1)) * 56 + 28 * kh) * 8;
    const u32x4 rQ = mkrsrc(ws + WS_Q + hd * (64 * 1792), (4 * art + 4) * 1792), rK = mkrsrc(ws + WS_K + hd * (64 * 1792), (4 * art + 4) * 1792), rV = mkrsrc(ws + WS_V + hd * (64 * 2048), 64 * 2048);
    const unsigned kv0 = tid2 * 16 + (4 * art - 8) * 1792, vv0 = tid2 * 16 + (4 * art - 8) * 2048;
    BAR();
    STAMP(4);
    dma16c<L_K>(kv0, rK, P.wk); dma16c<L_Q>(kv0 + 8 * 1792, rQ, P.wk); dma16c<L_K + UNIT>(kv0 + UNIT, rK, P.wk); dma16c<L_K + 2 * UNIT>(kv0 + 2 * UNIT, rK, P.wk);
    if ((lane2 & 31) < 28) { dma16c<L_V>(vv0, rV, P.wk); dma16c<L_V + UNIT>(vv0 + UNIT, rV, P.wk); dma16c<L_V + 2 * UNIT>(vv0 + 2 * UNIT, rV, P.wk); }
    const unsigned feoff = fhoff + (5 + (lane2 & 6)) * 16 + (lane2 & 1) * 8;
    u32x2 hfe = get_flag(rs, feoff);
    WAITV(8);
    put_flag(rs, fhw + fb * 16, ep);
    PC C; C.rw2 = rw2; C.rwo = mkrsrc(wo, 448 * 896); C.rs = rs; C.ep = ep; C.v = wv3; C.wk = P.wk;
    C.hbase = WS_H + ((4 * rt64 + tt3) * 56 + 28 * kh) * 1024 + lane2 * 16; C.fo = WS_FO + ((8 * art + wv) * 16 + hd) * 8 + (lane2 == 0 ? 0 : OOB);
    const unsigned oxo = (rt64 * 64 + (tid2 >> 3)) * 896 + cb * 128 + (tid2 & 7) * 16;
    const unsigned ka0 = L_K + (wv >> 1) * 1792 + p4b * 448 + (8 * gb + q4b) * 16 + ((gb & 1) << 3), ka1 = ka0 ^ 8;
    const unsigned qa = (ka0 - (L_K - L_Q)) ^ ((wv & 1) << 3);
    const unsigned va0 = L_V + (wv >> 1) * 2048 + gb * 512 + r16b * 16;
    const unsigned va1 = L_V + (wv >> 1) * 2048 + (p4b == 3 ? 28 * 16 : gb * 512 + (16 + 3 * q4b + p4b) * 16);
    const unsigned floff = fhoff + min(lane2, 25u) * 8;
    WAITBAR(1);
    STAMP(5);
    u32x4 hf[28];
    u32x2 hfl;
    const u32x4 xres = *(const GAS u32x4*)(x + oxo);
    {
        HalfFrag qh; rd_tr(qh, qa, 0, 64);
        HalfFrag kf[18];
#pragma unroll
        for (int t = 0; t < 4; ++t) rd_tr(kf[t], (t & 1) ? ka1 : ka0, (t >> 1) * 1792, 64);
        PINV(hfe);
        if (!all_set(hfe, ep)) spin_flags(rs, feoff, ep);
        if (gb == 3) qh.hi = (s16x4){0, 0, 0, 0};
        const bf16x8 qf = join(qh);
        f32x4 s[18];
        bf16x8 vf[9][2];
#pragma unroll
        for (int t = 0; t < 18; ++t) {
            s[t] = MFMA(join(kf[t]), qf, ZERO4);
            if (t + 4 < 18) rd_tr(kf[t + 4], (t & 1) ? ka1 : ka0, ((t + 4) >> 1) * 1792, 64);
            else if (t + 4 < 20) { vf[t - 14][0] = lds128(va0 + (t - 14) * 2048); vf[t - 14][1] = lds128(va1 + (t - 14) * 2048); }
            if (t == 5) ld_h<0>(hf, C);
            if (t == 11) ld_h<1>(hf, C);
            SB();
        }
        const unsigned dq = 256 + 16 * (wv & 1) + r16b - 4 * gb;
#pragma unroll
        for (int t = 0; t < 18; ++t) {
            if (t < 2 || t >= 16) {
#pragma unroll
                for (int j = 0; j < 4; ++j) { const unsigned dd = dq - 16 * t - j; s[t][j] = dd < 256u ? s[t][j] : -INFINITY; }
            }
        }
        if (art < 2) {
            const int kmin = 16 - 8 * (int)art - 2 * (int)(wv >> 1);
#pragma unroll
            for (int t = 0; t < 16; ++t) if (t < kmin) s[t] = (f32x4){-INFINITY, -INFINITY, -INFINITY, -INFINITY};
        }
        float m0 = __builtin_fmaxf(s[2][0], s[2][1]), m1 = __builtin_fmaxf(s[2][2], s[2][3]);
#pragma unroll
        for (int u = 3; u < 20; ++u) { const int t = u < 18 ? u : u - 18; m0 = __builtin_fmaxf(__builtin_fmaxf(m0, s[t][0]), s[t][1]); m1 = __builtin_fmaxf(__builtin_fmaxf(m1, s[t][2]), s[t][3]); }
        const float SC = 0.27264391f;
        const float mc = -SC * redmax4(__builtin_fmaxf(m0, m1));
        STAMP(6);
        f32x4 o0 = ZERO4, o1 = ZERO4;
#pragma unroll
        for (int b = 0; b < 9; ++b) {
            if (b + 2 < 9) { vf[b + 2][0] = lds128(va0 + (b + 2) * 2048); vf[b + 2][1] = lds128(va1 + (b + 2) * 2048); }
            const f32x4 ea = s[2 * b] * SC + mc, eb = s[2 * b + 1] * SC + mc;
            const bf16x8 pf = asf((u32x4){pk2(__builtin_amdgcn_exp2f(ea[0]), __builtin_amdgcn_exp2f(ea[1])), pk2(__builtin_amdgcn_exp2f(ea[2]), __builtin_amdgcn_exp2f(ea[3])),
                                          pk2(__builtin_amdgcn_exp2f(eb[0]), __builtin_amdgcn_exp2f(eb[1])), pk2(__builtin_amdgcn_exp2f(eb[2]), __builtin_amdgcn_exp2f(eb[3]))});
            o0 = MFMA(vf[b][0], pf, o0); o1 = MFMA(vf[b][1], pf, o1);
            SB();
            if (b == 0) ld_h<2>(hf, C); if (b == 1) ld_h<3>(hf, C); if (b == 2) ld_h<4>(hf, C); if (b == 3) ld_h<5>(hf, C);
            if (b == 4) ld_h<6>(hf, C); if (b == 5) ld_h<7>(hf, C); if (b == 6) hfl = get_flag(rs, floff);
            SB();
        }
        const float il = __builtin_amdgcn_rcpf(o1[3]);
        const u32x4 of = {pk2(o0[0] * il, o0[1] * il), pk2(o0[2] * il, o0[3] * il), pk2(o1[0] * il, o1[1] * il), pk2(o1[2] * il, 0.f)};
        __builtin_amdgcn_raw_buffer_store_b128(of, rs, WS_O + ((8 * art + wv) * 16 + hd) * 1024 + lane2 * 16, 0, 16);
    }
    STAMP(7);
    if (spare) {
        WAITV(0);
        put_flag(rs, C.fo, ep);
        if (bid == 4 + 8 * 28) {
#pragma nounroll
            for (int i = 0; i < 4; ++i) spin_flags(rs, WS_FO + (tid2 * 4 + i) * 8, ep);
            BAR();
            if (tid2 == 0) put_flag(rs, WS_E, ep);
        }
    } else {
        unsigned wb[4];
        {
            const unsigned key3 = (2 * gb + (q4b >> 1)) & 3;
#pragma unroll
            for (unsigned nt = 0; nt < 4; ++nt) wb[nt] = kh * UNIT + (4 * gb + q4b) * 128 + ((nt ^ key3) << 5) + p4b * 8;
        }
        HalfFrag wf[2][2][4];
#pragma unroll
        for (int i = 0; i < 8; ++i) rd_c<0>(wf, i >> 2, i & 3, wb);
        PINV(hfl);
        if (!all_set(hfl, ep)) spin_flags(rs, floff, ep);
        ld_h<8>(hf, C); ld_h<9>(hf, C); ld_h<10>(hf, C); ld_h<11>(hf, C); ld_h<12>(hf, C); ld_h<13>(hf, C);
        f32x4 ac[4] = {ZERO4, ZERO4, ZERO4, ZERO4};
        SB();
        step_c<0>(hf, wf, ac, wb, C);
        STAMP(8);
        const unsigned foff = lane2 < 8 ? WS_FO + ((4 * rt64 + tt3) * 16 + 8 * kh + lane2) * 8 : fhoff + (26 + (lane2 & 1)) * 8;
        u32x2 ofl = get_flag(rs, foff);
        HalfFrag wof[8][4];
        {
            const unsigned d0 = 4 * gb + q4b, d1 = 16 + 3 * gb + min(q4b, 2u);
#pragma unroll
            for (unsigned nt = 0; nt < 4; ++nt) {
                unsigned a0 = L_REG + (kh * 224 + d0) * 128 + ((nt ^ ((d0 >> 1) & 3)) << 5) + p4b * 8;
                unsigned a1 = L_REG + (kh * 224 + d1) * 128 + ((nt ^ ((d1 >> 1) & 3)) << 5) + p4b * 8;
                PINV(a0); PINV(a1);
#pragma unroll
                for (int i = 0; i < 8; ++i) { wof[i][nt ^ ((i & 1) << 1)].lo = ldstr(a0 + i * 3584); wof[i][nt ^ ((i & 1) << 1)].hi = ldstr(a1 + i * 3584); }
            }
        }
        SB();
        if (!all_set(ofl, ep)) spin_flags(rs, foff, ep);
        STAMP(9);
        u32x4 of[8];
        const unsigned obase = WS_O + ((4 * rt64 + tt3) * 16 + 8 * kh) * 1024 + lane2 * 16;
#pragma unroll
        for (int i = 0; i < 8; ++i) of[i] = __builtin_amdgcn_raw_buffer_load_b128(rs, obase + (i & 3) * 1024, (i >> 2) * 4096, 16);
        ld_h<26>(hf, C); ld_h<27>(hf, C);
        SB();
#pragma unroll
        for (int i = 0; i < 8; ++i)
#pragma unroll
            for (int nt = 0; nt < 4; ++nt) ac[nt] = MFMA(join(wof[i][nt]), asf(of[i]), ac[nt]);
#pragma unroll
        for (int e = 0; e < 2; ++e)
#pragma unroll
            for (int nt = 0; nt < 4; ++nt) ac[nt] = MFMA(join(wf[1][e][nt]), asf(hf[26 + e]), ac[nt]);
#pragma unroll
        for (int nt = 0; nt < 4; ++nt) *(LAS f32x4*)(size_t)(L_RED + (wv * 16 + r16b) * RED_RS + nt * 64 + gb * 16) = ac[nt];
        BAR();
        STAMP(10);
        {
            const unsigned ra = L_RED + (tid2 >> 3) * RED_RS + (tid2 & 7) * 32;
            const f32x4 a0 = ldsf4(ra) + ldsf4(ra + 64 * RED_RS), a1 = ldsf4(ra + 16) + ldsf4(ra + 64 * RED_RS + 16);
            const u32x4 bb = __builtin_bit_cast(u32x4, lds128(L_B2 + (tid2 & 7) * 16));
            u32x4 res;
#pragma unroll
            for (int j = 0; j < 4; ++j) {
                const unsigned xj = xres[j], bj = bb[j];
                const float s0 = j < 2 ? a0[2 * j] : a1[2 * j - 4], s1_ = j < 2 ? a0[2 * j + 1] : a1[2 * j - 3];
                res[j] = pk2(s0 + bflo(xj) + bflo(bj), s1_ + bfhi(xj) + bfhi(bj));
            }
            asm volatile(".Lfk_end:");
            *(GAS u32x4*)(out + oxo) = res;
        }
    }
    STAMP(11);
}

extern "C" void kernel_launch(void* const* d_in, const int* in_sizes, int n_in, void* d_out, int out_size, void* d_ws, size_t ws_size, hipStream_t stream) {
    (void)in_sizes; (void)n_in; (void)out_size; (void)ws_size;
    (void)hipFuncSetAttribute((const void*)fused_k, hipFuncAttributeMaxDynamicSharedMemorySize, LDS_BYTES);
    fused_k<<<256, 512, LDS_BYTES, stream>>>((const char*)d_in[0], (const char*)d_in[1], (const char*)d_in[2], (const char*)d_in[4], (const char*)d_in[5],
                                             (const char*)d_in[6], (const char*)d_in[7], (char*)d_out, (char*)d_ws);
}
```

```cpp
#include <hip/hip_runtime.h>
#include <stdint.h>

typedef unsigned long long u64;
typedef short bf16x8 __attribute__((ext_vector_type(8)));
typedef short s16x4 __attribute__((ext_vector_type(4)));
typedef __bf16 b16x2 __attribute__((ext_vector_type(2)));
typedef float f32x4 __attribute__((ext_vector_type(4)));
typedef float f32x2 __attribute__((ext_vector_type(2)));
typedef unsigned u32x4 __attribute__((ext_vector_type(4)));
typedef unsigned u32x2 __attribute__((ext_vector_type(2)));
typedef __amdgpu_buffer_rsrc_t rsrc_t;
#define LAS __attribute__((address_space(3)))
#define GAS __attribute__((address_space(1)))
#define DI __device__ __forceinline__
#define RLX_AGENT __ATOMIC_RELAXED, __HIP_MEMORY_SCOPE_AGENT

constexpr unsigned WS_E = 0;
constexpr unsigned WS_FQ = 4096;
constexpr unsigned WS_FH = 32768;
constexpr unsigned WS_FO = 65536;
constexpr unsigned WS_GS = 98304;
constexpr unsigned WS_Q = 131072;
constexpr unsigned WS_K = WS_Q + 16 * 64 * 1792;
constexpr unsigned WS_V = WS_K + 16 * 64 * 1792;
constexpr unsigned WS_H = WS_V + 16 * 64 * 2048;
constexpr unsigned WS_O = WS_H + 128 * 56 * 1024;
constexpr unsigned WS_END = WS_O + 128 * 16 * 1024;

constexpr int UNIT = 8192;
constexpr int phys(int piece) { return (((piece % 19) + 3) % 19) * UNIT; }
constexpr int L_REG = 12 * UNIT;
constexpr int L_Q = L_REG, L_K = L_REG + UNIT, L_V = L_REG + 4 * UNIT;
constexpr int L_TB = 19 * UNIT;
constexpr int L_CS = L_TB, L_B1 = L_TB + 1024, L_B2 = L_B1 + 768, L_JUNK = L_TB + 2048;
constexpr int L_RED = 4 * UNIT, RED_RS = 272;
constexpr int LDS_BYTES = 163840;

#define AFLAG_AT 7
#define OFLAG_AT 2
constexpr unsigned SPIN_MAX = 400000;

#define MFMA(a, b, c) __builtin_amdgcn_mfma_f32_16x16x32_bf16((a), (b), (c), 0, 0, 0)
#define WAITBAR(N) asm volatile("s_waitcnt vmcnt(%0) lgkmcnt(0)\n\ts_barrier" ::"i"(N) : "memory")
#define BAR() asm volatile("s_waitcnt lgkmcnt(0)\n\ts_barrier" ::: "memory")
#define WAITV(N) asm volatile("s_waitcnt vmcnt(%0)" ::"i"(N) : "memory")
#define SB() __builtin_amdgcn_sched_barrier(0)
#define PINV(x) asm volatile("" : "+v"(x))
#define PINS(x) asm volatile("" : "+s"(x))
#define ZERO4 ((f32x4){0.f, 0.f, 0.f, 0.f})

DI u32x4 mkrsrc(const char* p, unsigned bytes) { const u64 a = (u64)p; return (u32x4){(unsigned)a, (unsigned)(a >> 32), bytes, 0x20000u}; }
template <int DST> DI void dma16(unsigned voff, u32x4 r, unsigned soff, unsigned wk) {
    asm volatile("s_add_u32 m0, %3, %4\n\ts_nop 0\n\tbuffer_load_dwordx4 %0, %1, %2 offen lds" ::"v"(voff), "s"(r), "s"(soff), "s"(wk), "i"(DST) : "memory", "scc");
}
template <int DST, int SOFF> DI void dma16i(unsigned voff, u32x4 r, unsigned wk) {
    unsigned t;
    asm volatile("s_add_u32 m0, %3, %4\n\ts_mov_b32 %0, %5\n\tbuffer_load_dwordx4 %1, %2, %0 offen lds" : "=&s"(t) : "v"(voff), "s"(r), "s"(wk), "i"(DST), "i"(SOFF) : "memory", "scc");
}
template <int DST> DI void dma16c(unsigned voff, u32x4 r, unsigned wk) {
    asm volatile("s_add_u32 m0, %2, %3\n\ts_nop 0\n\tbuffer_load_dwordx4 %0, %1, 0 offen sc1 lds" ::"v"(voff), "s"(r), "s"(wk), "i"(DST) : "memory", "scc");
}
DI void gdma4(unsigned voff, const char* sbase, unsigned ldsdst) {
    asm volatile("s_mov_b32 m0, %2\n\ts_nop 0\n\tglobal_load_lds_dword %0, %1" ::"v"(voff), "s"(sbase), "s"(ldsdst) : "memory");
}
DI bf16x8 lds128(unsigned a) { return *(const LAS bf16x8*)(size_t)a; }
DI f32x4 ldsf4(unsigned a) { return *(const LAS f32x4*)(size_t)a; }
DI float ldsf(unsigned a) { return *(const LAS float*)(size_t)a; }
DI s16x4 ldstr(unsigned a) { return __builtin_amdgcn_ds_read_tr16_b64_v4i16((LAS s16x4*)(size_t)a); }
DI unsigned pk2(float a, float b) { f32x2 p = {a, b}; return __builtin_bit_cast(unsigned, __builtin_convertvector(p, b16x2)); }
DI float rbf(float a) { return __uint_as_float(pk2(a, 0.f) << 16); }
DI float bflo(unsigned u) { return __uint_as_float(u << 16); }
DI float bfhi(unsigned u) { return __uint_as_float(u & 0xffff0000u); }
DI bf16x8 asf(u32x4 v) { return __builtin_bit_cast(bf16x8, v); }
DI float redmax4(float v) {
    auto a = __builtin_amdgcn_permlane16_swap(__float_as_uint(v), __float_as_uint(v), false, false); v = __builtin_fmaxf(__uint_as_float(a[0]), __uint_as_float(a[1]));
    auto b = __builtin_amdgcn_permlane32_swap(__float_as_uint(v), __float_as_uint(v), false, false); return __builtin_fmaxf(__uint_as_float(b[0]), __uint_as_float(b[1])); }
template <int CTRL> DI float dppadd(float v) { return v + __uint_as_float(__builtin_amdgcn_update_dpp(0, __float_as_uint(v), CTRL, 0xf, 0xf, true)); }
DI float wavesum(float v) {
    v = dppadd<0xB1>(v); v = dppadd<0x4E>(v); v = dppadd<0x141>(v); v = dppadd<0x140>(v);
    auto a = __builtin_amdgcn_permlane16_swap(__float_as_uint(v), __float_as_uint(v), false, false); v = __uint_as_float(a[0]) + __uint_as_float(a[1]);
    auto b = __builtin_amdgcn_permlane32_swap(__float_as_uint(v), __float_as_uint(v), false, false); return __uint_as_float(b[0]) + __uint_as_float(b[1]); }
DI void put_flag(rsrc_t rs, unsigned off, u32x2 v) { __builtin_amdgcn_raw_buffer_store_b64(v, rs, off, 0, 16); }
DI u32x2 get_flag(rsrc_t rs, unsigned off) { return __builtin_amdgcn_raw_buffer_load_b64(rs, off, 0, 16); }
DI bool all_set(u32x2 f, u32x2 ep) { return __builtin_amdgcn_ballot_w64(((f[0] ^ ep[0]) | (f[1] ^ ep[1])) != 0) == 0; }
DI void spin_flags(rsrc_t rs, unsigned off, u32x2 ep) {
#pragma nounroll
    for (unsigned it = 0; it < SPIN_MAX; ++it) {
        asm volatile("" ::: "memory");
        if (all_set(get_flag(rs, off), ep)) break;
        __builtin_amdgcn_s_sleep(1);
    }
    asm volatile("" ::: "memory");
}
DI unsigned wrap7(unsigned v) { return v >= 7 ? v - 7 : v; }
constexpr int blk_at(int j) { return j < 4 ? 5 + 2 * j : (j < 9 ? j - 4 : (j < 13 ? 2 * j - 12 : 13)); }
constexpr int n_r(int i) { return (i >= 0 && i <= 7) ? 2 : 0; }
constexpr int n_h(int i) { return (i >= 0 && i <= 5) ? 2 : 0; }
constexpr int n_wo(int i) { return (i >= 6 && i <= 8) ? 2 : (i == 9 ? 1 : 0); }
constexpr int n_f(int i) { return i == OFLAG_AT ? 1 : 0; }
constexpr int cnt_w(int j) { if (j == 12) return 0; int c = n_h(j - 5) + n_wo(j - 5) + n_f(j - 5); for (int i = j - 4; i < j; ++i) c += n_r(i) + n_h(i) + n_wo(i) + n_f(i); return c; }
constexpr int cnt_o() { int c = 6; for (int i = 0; i <= OFLAG_AT; ++i) c += n_r(i) + n_h(i) + n_wo(i); return c; }

#define STAMP(i) do { } while (0)
#define DIAGPAR
#define DIAGARG

struct HalfFrag { s16x4 lo, hi; };
DI bf16x8 join(const HalfFrag& h) { return __builtin_shufflevector(h.lo, h.hi, 0, 1, 2, 3, 4, 5, 6, 7); }
DI void rd_tr(HalfFrag& h, unsigned base, unsigned c, unsigned d) {
    if (c + d < 65536) { h.lo = ldstr(base + c); h.hi = ldstr(base + c + d); }
    else { unsigned a = base + c; PINV(a); h.lo = ldstr(a); h.hi = ldstr(a + d); }
}

struct Gran { u32x2 f[2]; u32x4 q[2][2]; };
struct P1 {
    unsigned xa[2], wa[2][2];
    u32x4 rx, rq, r1, rw;
    unsigned xv0, xv1, v0, v1, v2;
    unsigned m1, wk, c0;
    rsrc_t rs; unsigned gf, gq, gs, tag, lane;
    u32x4 srow; float sv[8], sm, sq;
};
template <int N> DI void piece(const P1& P) {
    if constexpr (N < 28) {
        const unsigned kc = wrap7(P.c0 + (N >> 2)) * 128;
        if constexpr ((N & 3) == 0) dma16<phys(N)>(P.xv0, P.rx, kc, P.wk);
        else if constexpr ((N & 3) == 1) dma16<phys(N)>(P.xv1, P.rx, kc, P.wk);
        else if constexpr ((N & 3) == 2) dma16<phys(N)>(P.v0, P.rq, kc * 1344, P.wk);
        else dma16<phys(N)>(P.v1, P.r1, kc * P.m1, P.wk);
    } else if constexpr (N < 35) {
        dma16<phys(N)>(P.v2, P.rw, wrap7(P.c0 + (N - 28)) * (128 * 1792), P.wk);
    }
}
template <int S> DI void rd_ax(bf16x8 (&xf)[14][2], int tt, const P1& P) { if constexpr (S < 14) xf[S][tt] = lds128(P.xa[S & 1] + phys(4 * (S >> 1)) + tt * 2048); }
template <int S> DI void rd_aw(HalfFrag (&wf)[2][2][2], int b, int nt, const P1& P) { if constexpr (S < 14) rd_tr(wf[S & 1][b][nt], P.wa[S & 1][nt], phys(4 * (S >> 1) + 2 + b), 512); }
constexpr int a_first(int s) { return s == 0 ? 8 : (s == 1 ? 12 : (s == 2 ? 16 : 14 + 2 * s)); }
constexpr int a_count(int s) { return s <= 2 ? 4 : 2; }
DI void ld_gran(Gran& R, const P1& P) {
#pragma unroll
    for (int tt = 0; tt < 2; ++tt) {
        R.f[tt] = __builtin_amdgcn_raw_buffer_load_b64(P.rs, P.gf + tt * 128, 0, 16);
        R.q[tt][0] = __builtin_amdgcn_raw_buffer_load_b128(P.rs, P.gq + tt * 128, 0, 16); R.q[tt][1] = __builtin_amdgcn_raw_buffer_load_b128(P.rs, P.gq + tt * 128 + 16, 0, 16);
    }
}
template <int S> DI void stats_stage(P1& P) {
    if constexpr (S == 0) {
#pragma unroll
        for (int j = 0; j < 4; ++j) { const unsigned u = P.srow[j]; P.sv[2 * j] = bflo(u); P.sv[2 * j + 1] = bfhi(u); }
        P.sm = ((P.sv[0] + P.sv[1]) + (P.sv[2] + P.sv[3])) + ((P.sv[4] + P.sv[5]) + (P.sv[6] + P.sv[7])); PINV(P.sm);
    } else if constexpr (S == 1) { P.sm = wavesum(P.sm) * (1.f / 448.f); PINV(P.sm);
    } else if constexpr (S == 2) {
        float q0 = 0.f, q1 = 0.f;
#pragma unroll
        for (int j = 0; j < 4; ++j) { const float d0 = P.sv[j] - P.sm, d1 = P.sv[j + 4] - P.sm; q0 = __builtin_fmaf(d0, d0, q0); q1 = __builtin_fmaf(d1, d1, q1); }
        P.sq = P.lane < 56 ? q0 + q1 : 0.f; PINV(P.sq);
    } else if constexpr (S == 3) { P.sq = wavesum(P.sq) * (1.f / 448.f); PINV(P.sq);
    } else if constexpr (S == 4) {
        const unsigned sdm = pk2(__builtin_amdgcn_sqrtf(rbf(rbf(P.sq) + 1e-5f)), P.sm);
        put_flag(P.rs, P.gs, (u32x2){sdm, P.tag});
    }
}
template <bool B1Q, int S>
DI void step_a(bf16x8 (&xf)[14][2], HalfFrag (&wf)[2][2][2], f32x4 (&acc)[2][2][2], f32x4 (&cs)[2], bf16x8 ones, P1& P, Gran& R) {
    if constexpr ((S & 1) && S < 13) {
        constexpr int c = (S + 1) >> 1;
        if constexpr (c == 1) WAITBAR(4); else if constexpr (c == 2) WAITBAR(8); else if constexpr (c < 6) WAITBAR(9); else WAITBAR(7);
    }
    constexpr int f = a_first(S), n = a_count(S);
#define MA(b, nt, tt) do { acc[b][nt][tt] = (b == 0 || B1Q) ? MFMA(xf[S][tt], join(wf[S & 1][b][nt]), acc[b][nt][tt]) : MFMA(join(wf[S & 1][b][nt]), xf[S][tt], acc[b][nt][tt]); } while (0)
    MA(0, 0, 0); rd_ax<S + 1>(xf, 0, P); SB();
    MA(0, 0, 1); rd_aw<S + 1>(wf, 0, 0, P); SB();
    MA(0, 1, 0); piece<f>(P); SB();
    MA(0, 1, 1); rd_ax<S + 1>(xf, 1, P); SB();
    if constexpr (n > 2) { piece<f + 2>(P); SB(); }
    MA(1, 0, 0); rd_aw<S + 1>(wf, 0, 1, P); SB();
    MA(1, 0, 1); piece<f + 1>(P); SB();
    MA(1, 1, 0); rd_aw<S + 1>(wf, 1, 0, P); SB();
    MA(1, 1, 1); rd_aw<S + 1>(wf, 1, 1, P); SB();
#undef MA
    if constexpr ((S & 1) == 0) { cs[0] = MFMA(ones, join(wf[0][0][0]), cs[0]); cs[1] = MFMA(ones, join(wf[0][1][0]), cs[1]); SB(); }
    if constexpr (n > 2) { piece<f + 3>(P); SB(); }
    if constexpr (S <= 4) { stats_stage<S>(P); SB(); }
    if constexpr (S == 11) { ld_gran(R, P); SB(); }
    if constexpr (S < 13) step_a<B1Q, S + 1>(xf, wf, acc, cs, ones, P, R);
}
template <bool B1Q>
DI void pass_a(bf16x8 (&xf)[14][2], f32x4 (&acc)[2][2][2], f32x4 (&cs)[2], bf16x8 ones, P1& P, Gran& R DIAGPAR) {
    HalfFrag wf[2][2][2];
    STAMP(13);
    rd_ax<0>(xf, 0, P); rd_aw<0>(wf, 0, 0, P); rd_ax<0>(xf, 1, P); rd_aw<0>(wf, 0, 1, P); rd_aw<0>(wf, 1, 0, P); rd_aw<0>(wf, 1, 1, P); SB();
    step_a<B1Q, 0>(xf, wf, acc, cs, ones, P, R);
}
DI unsigned qkv_dest(unsigned n, unsigned tb, unsigned g) {
    const unsigned hs = (n * 2341u) >> 16, d = n - hs * 28u;
    const unsigned isv = hs >> 5, pl = 448 + 64 * isv;
    unsigned o = WS_Q - isv * (32 * 64 * 256) + (hs * 256 + tb * 4 + g) * pl + d * 16 + ((d >> 3) & 1 & ~isv);
    PINV(o); return o;
}
DI void epi_qkv(const f32x4 (&acc)[2][2], const f32x4 (&mr)[2], const f32x4 (&rq)[2], const float (&cs)[2], const unsigned (&q)[2], rsrc_t rs) {
#pragma unroll
    for (int nt = 0; nt < 2; ++nt) {
        unsigned w[2][2];
#pragma unroll
        for (int tt = 0; tt < 2; ++tt) {
            const f32x4 v = acc[nt][tt] * rq[tt] - mr[tt] * cs[nt];
            w[tt][0] = pk2(v[0], v[1]); w[tt][1] = pk2(v[2], v[3]);
        }
        const bool sw = q[nt] & 1;
        const u32x4 o = {sw ? w[1][0] : w[0][0], sw ? w[1][1] : w[0][1], sw ? w[0][0] : w[1][0], sw ? w[0][1] : w[1][1]};
        __builtin_amdgcn_raw_buffer_store_b128(o, rs, q[nt] & ~15u, 0, 16);
    }
}
DI void epi_ffn(const f32x4 (&acc)[2][2], u32x4 (&o)[2], const unsigned (&ta)[2], int slot, int b1i, bool s1, const float (&mr)[2], const float (&rstd)[2]) {
    f32x4 cs[2], bb[2];
#pragma unroll
    for (int nt = 0; nt < 2; ++nt) {
        cs[nt] = ldsf4(L_CS + (slot & 1) * 512 + ta[nt] * 2) + ldsf4(L_CS + (slot & 1) * 512 + 256 + ta[nt] * 2);
        const u32x2 b = *(const LAS u32x2*)(size_t)(L_B1 + b1i * 256 + ta[nt]);
        bb[nt] = (f32x4){bflo(b[0]), bfhi(b[0]), bflo(b[1]), bfhi(b[1])};
    }
#pragma unroll
    for (int tt = 0; tt < 2; ++tt) {
        unsigned w[2][2];
#pragma unroll
        for (int nt = 0; nt < 2; ++nt) {
            f32x4 u = acc[nt][tt] * rstd[tt] + (bb[nt] - mr[tt] * cs[nt]);
            u = __builtin_elementwise_max(u, ZERO4); u = u * u;
            w[nt][0] = pk2(u[0], u[1]); w[nt][1] = pk2(u[2], u[3]);
        }
        o[tt] = s1 ? (u32x4){w[1][0], w[1][1], w[0][0], w[0][1]} : (u32x4){w[0][0], w[0][1], w[1][0], w[1][1]};
    }
}
template <int S> DI void rd_b(HalfFrag (&wf)[3][2], int nt, const unsigned (&wa)[2][2]) { if constexpr (S < 14) rd_tr(wf[S % 3][nt], wa[S & 1][nt], (S >> 1) * UNIT, 512); }
struct PB { u32x4 r; rsrc_t rs; u32x2 ep; unsigned v, wk, f0, f1, c0; };
template <int MODE, int S>
DI void step_b(const bf16x8 (&xf)[14][2], HalfFrag (&wf)[3][2], f32x4 (&ac)[2][2], f32x4& cs, bf16x8 ones, const unsigned (&wa)[2][2], const PB& B) {
    ac[0][0] = MFMA(join(wf[S % 3][0]), xf[S][0], ac[0][0]); rd_b<S + 2>(wf, 0, wa); SB();
    ac[0][1] = MFMA(join(wf[S % 3][0]), xf[S][1], ac[0][1]); SB();
    if constexpr (MODE == 0 && S < 12) { dma16i<S * UNIT, (blk_at(S >> 1) + 14 * (S & 1)) * (64 * 896)>(B.v, B.r, B.wk); SB(); }
    if constexpr (MODE == 1 && S < 7) { dma16<S * UNIT>(B.v, B.r, wrap7(B.c0 + S) * (128 * 1792), B.wk); SB(); }
    ac[1][0] = MFMA(join(wf[S % 3][1]), xf[S][0], ac[1][0]); rd_b<S + 2>(wf, 1, wa); SB();
    ac[1][1] = MFMA(join(wf[S % 3][1]), xf[S][1], ac[1][1]);
    if constexpr ((S & 1) == 0) cs = MFMA(ones, join(wf[S % 3][0]), cs);
    SB();
    if constexpr (S == AFLAG_AT) {
        WAITV(MODE == 0 ? (S + 1 < 12 ? S + 1 : 12) : (MODE == 1 ? (S + 1 < 7 ? S + 1 : 7) : 0));
        put_flag(B.rs, B.f0, B.ep);
        if constexpr (MODE < 2) put_flag(B.rs, B.f1, B.ep);
        SB();
    }
    if constexpr (S < 13) step_b<MODE, S + 1>(xf, wf, ac, cs, ones, wa, B);
}
template <int MODE>
DI void pass_b(const bf16x8 (&xf)[14][2], u32x4 (&o)[2], bf16x8 ones, const P1& P, const PB& B, unsigned csw, const unsigned (&ta)[2], bool s1, const float (&mr)[2], const float (&rstd)[2]) {
    unsigned wa[2][2];
#pragma unroll
    for (int e = 0; e < 2; ++e)
#pragma unroll
        for (int nt = 0; nt < 2; ++nt) { wa[e][nt] = P.wa[e][nt] + (MODE < 2 ? L_REG : 0); PINV(wa[e][nt]); }
    f32x4 ac[2][2] = {{ZERO4, ZERO4}, {ZERO4, ZERO4}}, c2 = ZERO4;
    HalfFrag wf[3][2];
    rd_b<0>(wf, 0, wa); rd_b<0>(wf, 1, wa); rd_b<1>(wf, 0, wa); rd_b<1>(wf, 1, wa); SB();
    step_b<MODE, 0>(xf, wf, ac, c2, ones, wa, B);
    *(LAS float*)(size_t)(csw + (MODE == 2 ? 512 : 0)) = c2[0];
    BAR();
    epi_ffn(ac, o, ta, MODE == 2 ? 1 : 0, MODE == 2 ? 0 : 1, s1, mr, rstd);
}

template <int J> DI void rd_c(HalfFrag (&wf)[2][2][4], int e, int nt, const unsigned (&wb)[4]) { rd_tr(wf[J & 1][e][nt], wb[nt], ((2 * J) % 12) * UNIT + e * 4096, 2048); }
struct PC { u32x4 rw2, rwo; rsrc_t rs; u32x2 ep; unsigned v, wk, hbase, fo; };
template <int I> DI void ld_h(u32x4 (&hf)[28], const PC& C) { hf[I] = __builtin_amdgcn_raw_buffer_load_b128(C.rs, C.hbase + (I & 1) * 1024, blk_at(I >> 1) * 2048, 16); }
template <int J>
DI void step_c(u32x4 (&hf)[28], HalfFrag (&wf)[2][2][4], f32x4 (&ac)[4], const unsigned (&wb)[4], const PC& C) {
    if constexpr (J >= 5) WAITBAR(cnt_w(J)); else BAR();
#define MC(e, nt) do { ac[nt] = MFMA(join(wf[J & 1][e][nt]), asf(hf[2 * J + e]), ac[nt]); rd_c<J + 1>(wf, e, nt, wb); SB(); } while (0)
    MC(0, 0); MC(0, 1);
    if constexpr (J <= 7) { dma16i<((2 * J) % 12) * UNIT, blk_at(J + 6) * (64 * 896)>(C.v, C.rw2, C.wk); SB(); }
    MC(0, 2); MC(0, 3);
    if constexpr (J <= 7) { dma16i<((2 * J + 1) % 12) * UNIT, (blk_at(J + 6) + 14) * (64 * 896)>(C.v, C.rw2, C.wk); SB(); }
    MC(1, 0); MC(1, 1);
    if constexpr (J <= 5) { ld_h<2 * (J + 7)>(hf, C); SB(); }
    if constexpr (J >= 6 && J <= 9) { dma16i<L_REG + (2 * (J - 6)) * UNIT, (2 * (J - 6)) * (64 * 896)>(C.v, C.rwo, C.wk); SB(); }
    MC(1, 2); MC(1, 3);
#undef MC
    if constexpr (J <= 5) { ld_h<2 * (J + 7) + 1>(hf, C); SB(); }
    if constexpr (J >= 6 && J < 9) { dma16i<L_REG + (2 * (J - 6) + 1) * UNIT, (2 * (J - 6) + 1) * (64 * 896)>(C.v, C.rwo, C.wk); SB(); }
    if constexpr (J == OFLAG_AT) { WAITV(cnt_o()); put_flag(C.rs, C.fo, C.ep); SB(); }
    if constexpr (J < 12) step_c<J + 1>(hf, wf, ac, wb, C);
}

constexpr unsigned OOB = 0x80000000u;

__global__ void __launch_bounds__(512, 2) fused_k(const char* __restrict__ x, const char* __restrict__ wqkv, const char* __restrict__ wo, const char* __restrict__ w1,
                                                  const char* __restrict__ b1, const char* __restrict__ w2, const char* __restrict__ b2, char* __restrict__ out, char* ws) {
    STAMP(0);
    const u64 amp0 = __builtin_amdgcn_s_memrealtime();
    const unsigned tid = threadIdx.x, lane = tid & 63, wv = __builtin_amdgcn_readfirstlane(tid >> 6);
    const unsigned bid = blockIdx.x, xcd = bid & 7, idx = bid >> 3;
    {
        u64 pc; unsigned len;
        asm volatile("s_getpc_b64 %0\n.Lfk_begin:\n\ts_mov_b32 %1, .Lfk_end-.Lfk_begin-256" : "=s"(pc), "=s"(len));
        gdma4(lane * 4, (const char*)(pc + min(idx * 1024 + wv * 256, len)), L_JUNK + wv * 256);
    }
    const unsigned rt = (xcd & 1) * 8 + (idx & 7), G = xcd >> 1, slot = G * 4 + (idx >> 3);
    const bool two = slot < 5;
    const unsigned qb0 = two ? 2 * slot : slot + 5;
    const unsigned blk1 = two ? 2 * slot + 1 : 2 * slot - 5;
    const unsigned fb2 = two ? slot : 2 * slot - 4;
    const unsigned drow = tid >> 3, dc = lane & 7;
    P1 P;
    P.wk = wv * 1024; P.c0 = (rt + slot) - 7 * (((rt + slot) * 37) >> 8); P.lane = lane;
    P.xv0 = (rt * 128 + drow) * 896 + ((dc ^ ((drow >> 1) & 7)) << 4); P.xv1 = P.xv0 + 64 * 896;
    const unsigned wsw1 = (dc ^ ((drow & 2) | ((drow >> 1) & 4))) << 4;
    P.v0 = drow * 2688 + wsw1 + qb0 * 128; P.v2 = drow * 3584 + wsw1;
    P.m1 = two ? 1344 : 1792; P.v1 = drow * 2 * P.m1 + wsw1 + blk1 * 128;
    const unsigned srow_off = (rt * 128 + slot * 8 + wv) * 896 + lane * 16 + (lane < 56 ? 0 : OOB);
    asm volatile("" : "+s"(x), "+s"(wqkv), "+s"(wo), "+s"(w1), "+s"(b1), "+s"(w2), "+s"(b2), "+s"(out), "+s"(ws));
    P.rx = mkrsrc(x, 2048 * 896); P.rq = mkrsrc(wqkv, 448 * 2688); P.rw = mkrsrc(w1, 448 * 3584); P.r1 = two ? P.rq : P.rw;
    const u64 e0 = *(const __attribute__((address_space(4))) u64*)(ws + WS_E);
    asm volatile("buffer_load_dwordx4 %0, %1, %2, 0 offen" : "=v"(P.srow) : "v"(srow_off), "s"(P.rx) : "memory");
    piece<0>(P); piece<1>(P); piece<2>(P); piece<3>(P);
    const bool spare = idx - (G == 0 ? 24u : 28u) < 4u;
    const bool extra = spare && G < 2;
    const unsigned p3 = spare ? 0 : (idx >= 28 ? idx - 4 : idx);
    const unsigned p37 = (p3 * 37) >> 8, rt64 = 4 * xcd + p37, cb = p3 - 7 * p37;
    {
        const unsigned w3 = wv & 3, sel = 27u | (fb2 << 8) | ((blk1 & 31) << 16) | (cb << 24);
        gdma4((lane & 31) * 4 + ((sel >> (8 * w3)) & 31) * 128, w3 == 3 ? b2 : b1, L_B1 + w3 * 256 + (wv >> 2) * 4096);
    }
    piece<4>(P); piece<5>(P); piece<6>(P); piece<7>(P);
    const unsigned vx27 = P.v2 + 27 * 128;
    P.v2 += fb2 * 128;
    STAMP(12);
    const unsigned r16 = lane & 15, g = lane >> 4, q4 = r16 >> 2, p4 = lane & 3;
    const unsigned slab = wv & 3, ch = wv >> 2, s1 = slab >> 1;
    const u64 ep64 = e0 * 0xD1342543DE82EF95ull + 0x9E3779B97F4A7C15ull;
    const u32x2 ep = {(unsigned)ep64, (unsigned)(ep64 >> 32)};
    const rsrc_t rs = __builtin_amdgcn_make_buffer_rsrc(ws, 0, (int)WS_END, 0x00020000);
    const unsigned l0 = lane == 0 ? 0 : OOB;
    {
        const unsigned fl = r16 >> 1, key = ((q4 >> 1) & 1) | ((g & 1) << 1);
#pragma unroll
        for (unsigned e = 0; e < 2; ++e) {
            const unsigned ee = e ^ ch ^ (slab & 1);
            P.xa[e] = (slab >> 1) * UNIT + ((slab & 1) * 32 + r16) * 128 + (((4 * ee + g) ^ fl) << 4);
#pragma unroll
            for (unsigned nt = 0; nt < 2; ++nt) P.wa[e][nt] = ee * 4096 + (8 * g + q4) * 128 + (((2 * ch + (nt ^ s1)) ^ key) << 5) + p4 * 8;
        }
    }
    const unsigned tb = 4 * rt + slab;
    P.rs = rs; P.tag = ep[1]; P.gf = WS_GS + (tb * 32 + r16) * 8; P.gq = WS_GS + (tb * 32 + 4 * g) * 8; P.gs = WS_GS + (rt * 128 + slot * 8 + wv) * 8 + l0;
    unsigned qd[2][2], ta[2];
#pragma unroll
    for (unsigned nt = 0; nt < 2; ++nt) {
        const unsigned nl = 32 * ch + 16 * (nt ^ s1);
        qd[0][nt] = qkv_dest(64 * qb0 + nl + r16, tb, g); qd[1][nt] = qkv_dest(64 * (two ? blk1 : qb0) + nl + r16, tb, g);
        ta[nt] = (nl + 4 * g) * 2;
    }
    u32x4 ones4 = {0x3f803f80u, 0x3f803f80u, 0x3f803f80u, 0x3f803f80u}; PINV(ones4);
    const bf16x8 ones = asf(ones4);
    bf16x8 xf[14][2];
    f32x4 acc[2][2][2], cs[2] = {ZERO4, ZERO4};
#pragma unroll
    for (int b = 0; b < 2; ++b)
#pragma unroll
        for (int nt = 0; nt < 2; ++nt)
#pragma unroll
            for (int tt = 0; tt < 2; ++tt) acc[b][nt][tt] = ZERO4;
    Gran R;
    asm volatile("s_waitcnt vmcnt(5) lgkmcnt(0)\n\ts_barrier" : "+v"(P.srow)::"memory");
    if (two) pass_a<true>(xf, acc, cs, ones, P, R DIAGARG);
    else pass_a<false>(xf, acc, cs, ones, P, R DIAGARG);
    STAMP(1);
    const unsigned csw = L_CS + (slab & 1) * 256 + (32 * ch + 16 * s1 + r16) * 4;
    *(LAS float*)(size_t)(csw) = cs[0][0];
    *(LAS float*)(size_t)(csw + 512) = cs[1][0];
#pragma nounroll
    for (unsigned it = 0; it < SPIN_MAX; ++it) {
        unsigned bad = (R.f[0][1] ^ P.tag) | (R.f[1][1] ^ P.tag);
#pragma unroll
        for (int i = 0; i < 4; ++i) bad |= (R.q[i >> 1][i & 1][1] ^ P.tag) | (R.q[i >> 1][i & 1][3] ^ P.tag);
        if (__builtin_amdgcn_ballot_w64(bad != 0) == 0) break;
        __builtin_amdgcn_s_sleep(1);
        asm volatile("" ::: "memory");
        ld_gran(R, P);
    }
    float mr[2], rstd[2], csq[2][2];
    f32x4 mq[2], rq[2];
#pragma unroll
    for (int tt = 0; tt < 2; ++tt) {
        rstd[tt] = __builtin_amdgcn_rcpf(bflo(R.f[tt][0])); mr[tt] = bfhi(R.f[tt][0]) * rstd[tt];
#pragma unroll
        for (int j = 0; j < 4; ++j) { const unsigned u = R.q[tt][j >> 1][2 * (j & 1)]; rq[tt][j] = __builtin_amdgcn_rcpf(bflo(u)); mq[tt][j] = bfhi(u); }
        mq[tt] *= rq[tt]; PINV(mq[tt]);
    }
    BAR();
#pragma unroll
    for (int b = 0; b < 2; ++b)
#pragma unroll
        for (int nt = 0; nt < 2; ++nt) { const unsigned a = (csw & ~(256u | 64u)) + b * 512 + (nt ^ s1) * 64; csq[b][nt] = ldsf(a) + ldsf(a + 256); }
    const unsigned hst = WS_H + (2 * tb * 56 + ch) * 1024 + lane * 16;
    epi_qkv(acc[0], mq, rq, csq[0], qd[0], rs);
    if (two) epi_qkv(acc[1], mq, rq, csq[1], qd[1], rs);
    else {
        u32x4 o[2]; epi_ffn(acc[1], o, ta, 1, 2, s1, mr, rstd);
#pragma unroll
        for (int tt = 0; tt < 2; ++tt) __builtin_amdgcn_raw_buffer_store_b128(o[tt], rs, hst + blk1 * 2048 + tt * 56 * 1024, 0, 16);
    }
    STAMP(2);
    const unsigned wv3 = drow * 896 + ((dc ^ (drow & 6)) << 4) + cb * 128;
    const u32x4 rw2 = mkrsrc(w2, 1792 * 896);
    const unsigned fhw = WS_FH + (tb * 56 + ch) * 8 + l0;
    PB B; B.rs = rs; B.ep = ep; B.wk = P.wk; B.c0 = P.c0;
    B.f0 = WS_FQ + ((2 * qb0 + ch) * 64 + tb) * 8 + l0;
    B.f1 = two ? B.f0 + 128 * 8 : fhw + blk1 * 16;
    const unsigned hd = 2 * xcd + (idx & 1), art = idx >> 1;
    u32x4 ob[2]; unsigned fb = fb2;
    WAITBAR(4);
    STAMP(15);
    if (!extra) { B.r = rw2; B.v = wv3; pass_b<0>(xf, ob, ones, P, B, csw, ta, s1, mr, rstd); }
    else {
        B.r = P.rw; B.v = vx27; pass_b<1>(xf, ob, ones, P, B, csw, ta, s1, mr, rstd);
#pragma unroll
        for (int tt = 0; tt < 2; ++tt) __builtin_amdgcn_raw_buffer_store_b128(ob[tt], rs, hst + fb2 * 2048 + tt * 56 * 1024, 0, 16);
        WAITBAR(AFLAG_AT >= 6 ? 4 : 2);
        B.f0 = fhw + fb2 * 16; fb = 27;
        pass_b<2>(xf, ob, ones, P, B, csw, ta, s1, mr, rstd);
    }
    STAMP(3);
    unsigned tid2 = threadIdx.x; PINV(tid2);
    const unsigned lane2 = tid2 & 63, r16b = lane2 & 15, gb = lane2 >> 4, q4b = r16b >> 2, p4b = lane2 & 3;
    if (wv == 0) {
        const unsigned sel = lane2 >= 28, m = min(lane2 - 28 * sel, 27u);
        const unsigned sec = (m >= 4) + (m >= 16);
        const int t = (int)(4 * art + m) - 12 * (int)sec;
        spin_flags(rs, WS_FQ + (((448 * sec + 28 * hd + 27 * sel) >> 5) * 64 + (t < 0 ? 4 * art : t)) * 8, ep);
    }
#pragma unroll
    for (int tt = 0; tt < 2; ++tt) __builtin_amdgcn_raw_buffer_store_b128(ob[tt], rs, hst + fb * 2048 + tt * 56 * 1024, 0, 16);
    if (tid2 < 12) *(LAS u32x4*)(size_t)(L_V + tid2 * 2048 + 28 * 16) = ones4;
    const unsigned tt3 = wv & 3, kh = wv >> 2;
    const unsigned fhoff = WS_FH + ((2 * rt64 + (tt3 >> 1)) * 56 + 28 * kh) * 8;
    const u32x4 rQ = mkrsrc(ws + WS_Q + hd * (64 * 1792), (4 * art + 4) * 1792), rK = mkrsrc(ws + WS_K + hd * (64 * 1792), (4 * art + 4) * 1792), rV = mkrsrc(ws + WS_V + hd * (64 * 2048), 64 * 2048);
    const unsigned kv0 = tid2 * 16 + (4 * art - 8) * 1792, vv0 = tid2 * 16 + (4 * art - 8) * 2048;
    BAR();
    STAMP(4);
    dma16c<L_K>(kv0, rK, P.wk); dma16c<L_Q>(kv0 + 8 * 1792, rQ, P.wk); dma16c<L_K + UNIT>(kv0 + UNIT, rK, P.wk); dma16c<L_K + 2 * UNIT>(kv0 + 2 * UNIT, rK, P.wk);
    if ((lane2 & 31) < 28) { dma16c<L_V>(vv0, rV, P.wk); dma16c<L_V + UNIT>(vv0 + UNIT, rV, P.wk); dma16c<L_V + 2 * UNIT>(vv0 + 2 * UNIT, rV, P.wk); }
    const unsigned feoff = fhoff + (5 + (lane2 & 6)) * 16 + (lane2 & 1) * 8;
    u32x2 hfe = get_flag(rs, feoff);
    WAITV(8);
    put_flag(rs, fhw + fb * 16, ep);
    PC C; C.rw2 = rw2; C.rwo = mkrsrc(wo, 448 * 896); C.rs = rs; C.ep = ep; C.v = wv3; C.wk = P.wk;
    C.hbase = WS_H + ((4 * rt64 + tt3) * 56 + 28 * kh) * 1024 + lane2 * 16; C.fo = WS_FO + ((8 * art + wv) * 16 + hd) * 8 + (lane2 == 0 ? 0 : OOB);
    const unsigned oxo = (rt64 * 64 + (tid2 >> 3)) * 896 + cb * 128 + (tid2 & 7) * 16;
    const unsigned ka0 = L_K + (wv >> 1) * 1792 + p4b * 448 + (8 * gb + q4b) * 16 + ((gb & 1) << 3), ka1 = ka0 ^ 8;
    const unsigned qa = (ka0 - (L_K - L_Q)) ^ ((wv & 1) << 3);
    const unsigned va0 = L_V + (wv >> 1) * 2048 + gb * 512 + r16b * 16;
    const unsigned va1 = L_V + (wv >> 1) * 2048 + (p4b == 3 ? 28 * 16 : gb * 512 + (16 + 3 * q4b + p4b) * 16);
    const unsigned floff = fhoff + min(lane2, 25u) * 8;
    WAITBAR(1);
    STAMP(5);
    u32x4 hf[28];
    u32x2 hfl;
    const u32x4 xres = *(const GAS u32x4*)(x + oxo);
    {
        HalfFrag qh; rd_tr(qh, qa, 0, 64);
        HalfFrag kf[18];
#pragma unroll
        for (int t = 0; t < 4; ++t) rd_tr(kf[t], (t & 1) ? ka1 : ka0, (t >> 1) * 1792, 64);
        PINV(hfe);
        if (!all_set(hfe, ep)) spin_flags(rs, feoff, ep);
        if (gb == 3) qh.hi = (s16x4){0, 0, 0, 0};
        const bf16x8 qf = join(qh);
        f32x4 s[18];
        bf16x8 vf[9][2];
#pragma unroll
        for (int t = 0; t < 18; ++t) {
            s[t] = MFMA(join(kf[t]), qf, ZERO4);
            if (t + 4 < 18) rd_tr(kf[t + 4], (t & 1) ? ka1 : ka0, ((t + 4) >> 1) * 1792, 64);
            else if (t + 4 < 20) { vf[t - 14][0] = lds128(va0 + (t - 14) * 2048); vf[t - 14][1] = lds128(va1 + (t - 14) * 2048); }
            if (t == 5) ld_h<0>(hf, C);
            if (t == 11) ld_h<1>(hf, C);
            SB();
        }
        const unsigned dq = 256 + 16 * (wv & 1) + r16b - 4 * gb;
#pragma unroll
        for (int t = 0; t < 18; ++t) {
            if (t < 2 || t >= 16) {
#pragma unroll
                for (int j = 0; j < 4; ++j) { const unsigned dd = dq - 16 * t - j; s[t][j] = dd < 256u ? s[t][j] : -INFINITY; }
            }
        }
        if (art < 2) {
            const int kmin = 16 - 8 * (int)art - 2 * (int)(wv >> 1);
#pragma unroll
            for (int t = 0; t < 16; ++t) if (t < kmin) s[t] = (f32x4){-INFINITY, -INFINITY, -INFINITY, -INFINITY};
        }
        float m0 = __builtin_fmaxf(s[2][0], s[2][1]), m1 = __builtin_fmaxf(s[2][2], s[2][3]);
#pragma unroll
        for (int u = 3; u < 20; ++u) { const int t = u < 18 ? u : u - 18; m0 = __builtin_fmaxf(__builtin_fmaxf(m0, s[t][0]), s[t][1]); m1 = __builtin_fmaxf(__builtin_fmaxf(m1, s[t][2]), s[t][3]); }
        const float SC = 0.27264391f;
        const float mc = -SC * redmax4(__builtin_fmaxf(m0, m1));
        STAMP(6);
        f32x4 o0 = ZERO4, o1 = ZERO4;
#pragma unroll
        for (int b = 0; b < 9; ++b) {
            if (b + 2 < 9) { vf[b + 2][0] = lds128(va0 + (b + 2) * 2048); vf[b + 2][1] = lds128(va1 + (b + 2) * 2048); }
            const f32x4 ea = s[2 * b] * SC + mc, eb = s[2 * b + 1] * SC + mc;
            const bf16x8 pf = asf((u32x4){pk2(__builtin_amdgcn_exp2f(ea[0]), __builtin_amdgcn_exp2f(ea[1])), pk2(__builtin_amdgcn_exp2f(ea[2]), __builtin_amdgcn_exp2f(ea[3])),
                                          pk2(__builtin_amdgcn_exp2f(eb[0]), __builtin_amdgcn_exp2f(eb[1])), pk2(__builtin_amdgcn_exp2f(eb[2]), __builtin_amdgcn_exp2f(eb[3]))});
            o0 = MFMA(vf[b][0], pf, o0); o1 = MFMA(vf[b][1], pf, o1);
            SB();
            if (b == 0) ld_h<2>(hf, C); if (b == 1) ld_h<3>(hf, C); if (b == 2) ld_h<4>(hf, C); if (b == 3) ld_h<5>(hf, C);
            if (b == 4) ld_h<6>(hf, C); if (b == 5) ld_h<7>(hf, C); if (b == 6) hfl = get_flag(rs, floff);
            SB();
        }
        const float il = __builtin_amdgcn_rcpf(o1[3]);
        const u32x4 of = {pk2(o0[0] * il, o0[1] * il), pk2(o0[2] * il, o0[3] * il), pk2(o1[0] * il, o1[1] * il), pk2(o1[2] * il, 0.f)};
        __builtin_amdgcn_raw_buffer_store_b128(of, rs, WS_O + ((8 * art + wv) * 16 + hd) * 1024 + lane2 * 16, 0, 16);
    }
    STAMP(7);
    if (spare) {
        WAITV(0);
        put_flag(rs, C.fo, ep);
        if (bid == 4 + 8 * 28) {
#pragma nounroll
            for (int i = 0; i < 4; ++i) spin_flags(rs, WS_FO + (tid2 * 4 + i) * 8, ep);
            BAR();
            if (tid2 == 0) put_flag(rs, WS_E, ep);
        }
    } else {
        unsigned wb[4];
        {
            const unsigned key3 = (2 * gb + (q4b >> 1)) & 3;
#pragma unroll
            for (unsigned nt = 0; nt < 4; ++nt) wb[nt] = kh * UNIT + (4 * gb + q4b) * 128 + ((nt ^ key3) << 5) + p4b * 8;
        }
        HalfFrag wf[2][2][4];
#pragma unroll
        for (int i = 0; i < 8; ++i) rd_c<0>(wf, i >> 2, i & 3, wb);
        PINV(hfl);
        if (!all_set(hfl, ep)) spin_flags(rs, floff, ep);
        ld_h<8>(hf, C); ld_h<9>(hf, C); ld_h<10>(hf, C); ld_h<11>(hf, C); ld_h<12>(hf, C); ld_h<13>(hf, C);
        f32x4 ac[4] = {ZERO4, ZERO4, ZERO4, ZERO4};
        SB();
        step_c<0>(hf, wf, ac, wb, C);
        STAMP(8);
        const unsigned foff = lane2 < 8 ? WS_FO + ((4 * rt64 + tt3) * 16 + 8 * kh + lane2) * 8 : fhoff + (26 + (lane2 & 1)) * 8;
        u32x2 ofl = get_flag(rs, foff);
        HalfFrag wof[8][4];
        {
            const unsigned d0 = 4 * gb + q4b, d1 = 16 + 3 * gb + min(q4b, 2u);
#pragma unroll
            for (unsigned nt = 0; nt < 4; ++nt) {
                unsigned a0 = L_REG + (kh * 224 + d0) * 128 + ((nt ^ ((d0 >> 1) & 3)) << 5) + p4b * 8;
                unsigned a1 = L_REG + (kh * 224 + d1) * 128 + ((nt ^ ((d1 >> 1) & 3)) << 5) + p4b * 8;
                PINV(a0); PINV(a1);
#pragma unroll
                for (int i = 0; i < 8; ++i) { wof[i][nt ^ ((i & 1) << 1)].lo = ldstr(a0 + i * 3584); wof[i][nt ^ ((i & 1) << 1)].hi = ldstr(a1 + i * 3584); }
            }
        }
        SB();
        if (!all_set(ofl, ep)) spin_flags(rs, foff, ep);
        STAMP(9);
        u32x4 of[8];
        const unsigned obase = WS_O + ((4 * rt64 + tt3) * 16 + 8 * kh) * 1024 + lane2 * 16;
#pragma unroll
        for (int i = 0; i < 8; ++i) of[i] = __builtin_amdgcn_raw_buffer_load_b128(rs, obase + (i & 3) * 1024, (i >> 2) * 4096, 16);
        ld_h<26>(hf, C); ld_h<27>(hf, C);
        SB();
#pragma unroll
        for (int i = 0; i < 8; ++i)
#pragma unroll
            for (int nt = 0; nt < 4; ++nt) ac[nt] = MFMA(join(wof[i][nt]), asf(of[i]), ac[nt]);
#pragma unroll
        for (int e = 0; e < 2; ++e)
#pragma unroll
            for (int nt = 0; nt < 4; ++nt) ac[nt] = MFMA(join(wf[1][e][nt]), asf(hf[26 + e]), ac[nt]);
#pragma unroll
        for (int nt = 0; nt < 4; ++nt) *(LAS f32x4*)(size_t)(L_RED + (wv * 16 + r16b) * RED_RS + nt * 64 + gb * 16) = ac[nt];
        BAR();
        STAMP(10);
        {
            const unsigned ra = L_RED + (tid2 >> 3) * RED_RS + (tid2 & 7) * 32;
            const f32x4 a0 = ldsf4(ra) + ldsf4(ra + 64 * RED_RS), a1 = ldsf4(ra + 16) + ldsf4(ra + 64 * RED_RS + 16);
            const u32x4 bb = __builtin_bit_cast(u32x4, lds128(L_B2 + (tid2 & 7) * 16));
            u32x4 res;
#pragma unroll
            for (int j = 0; j < 4; ++j) {
                const unsigned xj = xres[j], bj = bb[j];
                const float s0 = j < 2 ? a0[2 * j] : a1[2 * j - 4], s1_ = j < 2 ? a0[2 * j + 1] : a1[2 * j - 3];
                res[j] = pk2(s0 + bflo(xj) + bflo(bj), s1_ + bfhi(xj) + bfhi(bj));
            }
            asm volatile(".Lfk_end:");
            *(GAS u32x4*)(out + oxo) = res;
        }
    }
    STAMP(11);
    if (lane2 == 0) { *(GAS u64*)(ws + (40u << 20) + (bid * 8 + wv) * 16) = amp0; *(GAS u64*)(ws + (40u << 20) + (bid * 8 + wv) * 16 + 8) = __builtin_amdgcn_s_memrealtime(); }
}

__global__ void post_k(char* ws) {
    const u64 a1 = __builtin_amdgcn_s_memrealtime();
    u64 mn = ~0ull, mx = 0;
    for (int i = threadIdx.x; i < 2048; i += 64) { const u64 s0 = *(volatile GAS u64*)(ws + (40u << 20) + i * 16), e0 = *(volatile GAS u64*)(ws + (40u << 20) + i * 16 + 8); mn = s0 < mn ? s0 : mn; mx = e0 > mx ? e0 : mx; }
    for (int m = 32; m >= 1; m >>= 1) { u64 o = __shfl_xor(mn, m); mn = o < mn ? o : mn; o = __shfl_xor(mx, m); mx = o > mx ? o : mx; }
    const u64 until = a1 + (0) * 10; while (__builtin_amdgcn_s_memrealtime() < until) __builtin_amdgcn_s_sleep(4);
}
extern "C" void kernel_launch(void* const* d_in, const int* in_sizes, int n_in, void* d_out, int out_size, void* d_ws, size_t ws_size, hipStream_t stream) {
    (void)in_sizes; (void)n_in; (void)out_size; (void)ws_size;
    (void)hipFuncSetAttribute((const void*)fused_k, hipFuncAttributeMaxDynamicSharedMemorySize, LDS_BYTES);
    fused_k<<<256, 512, LDS_BYTES, stream>>>((const char*)d_in[0], (const char*)d_in[1], (const char*)d_in[2], (const char*)d_in[4], (const char*)d_in[5],
                                             (const char*)d_in[6], (const char*)d_in[7], (char*)d_out, (char*)d_ws);
    post_k<<<1, 64, 0, stream>>>((char*)d_ws);
}
```

```cpp
#include <hip/hip_runtime.h>
#include <stdint.h>

typedef unsigned long long u64;
typedef short bf16x8 __attribute__((ext_vector_type(8)));
typedef short s16x4 __attribute__((ext_vector_type(4)));
typedef __bf16 b16x2 __attribute__((ext_vector_type(2)));
typedef float f32x4 __attribute__((ext_vector_type(4)));
typedef float f32x2 __attribute__((ext_vector_type(2)));
typedef unsigned u32x4 __attribute__((ext_vector_type(4)));
typedef unsigned u32x2 __attribute__((ext_vector_type(2)));
typedef __amdgpu_buffer_rsrc_t rsrc_t;
#define LAS __attribute__((address_space(3)))
#define GAS __attribute__((address_space(1)))
#define DI __device__ __forceinline__
#define RLX_AGENT __ATOMIC_RELAXED, __HIP_MEMORY_SCOPE_AGENT

constexpr unsigned WS_E = 0;
constexpr unsigned WS_FQ = 4096;
constexpr unsigned WS_FH = 49152;
constexpr unsigned WS_FO = 106496;
constexpr unsigned WS_GS = 139264;
constexpr unsigned WS_Q = 163840;
constexpr unsigned WS_K = WS_Q + 16 * 64 * 1792;
constexpr unsigned WS_V = WS_K + 16 * 64 * 1792;
constexpr unsigned WS_H = WS_V + 16 * 64 * 2048;
constexpr unsigned WS_O = WS_H + 128 * 56 * 1024;
constexpr unsigned WS_END = WS_O + 128 * 16 * 1024;

constexpr int UNIT = 8192;
constexpr int phys(int piece) { return (((piece % 19) + 3) % 19) * UNIT; }
constexpr int L_REG = 12 * UNIT;
constexpr int L_Q = 0, L_K = UNIT, L_V = 4 * UNIT, L_WO = 0, L_WO2 = 15 * UNIT;
constexpr int L_TB = 19 * UNIT;
constexpr int L_CS = L_TB, L_B1 = L_TB + 1024, L_B2 = L_B1 + 768, L_JUNK = L_TB + 2048, L_ST = L_TB + 6144;
constexpr int L_RING = 7 * UNIT, L_CNT = L_TB + 4096, L_CS2 = L_TB + 4608;
constexpr int rslot(int ks) { return ((ks & 3) >> 1) * 4 * UNIT + (ks & 1) * 4096; }
constexpr int LDS_BYTES = 163840;

#define FQ_AT 3
#define HF1_AT 6
#define ESLP 4
#define SLP 4
#define OSLP 0
constexpr unsigned SPIN_MAX = 400000;
constexpr unsigned OOB = 0x80000000u;

#define MFMA(a, b, c) __builtin_amdgcn_mfma_f32_16x16x32_bf16((a), (b), (c), 0, 0, 0)
#define WAITBAR(N) asm volatile("s_waitcnt vmcnt(%0) lgkmcnt(0)\n\ts_barrier" ::"i"(N) : "memory")
#define BAR() asm volatile("s_waitcnt lgkmcnt(0)\n\ts_barrier" ::: "memory")
#define WAITV(N) asm volatile("s_waitcnt vmcnt(%0)" ::"i"(N) : "memory")
#define SB() __builtin_amdgcn_sched_barrier(0)
#define PINV(x) asm volatile("" : "+v"(x))
#define PINS(x) asm volatile("" : "+s"(x))
#define ZERO4 ((f32x4){0.f, 0.f, 0.f, 0.f})

DI u32x4 mkrsrc(const char* p, unsigned bytes) { const u64 a = (u64)p; return (u32x4){(unsigned)a, (unsigned)(a >> 32), bytes, 0x20000u}; }
template <int DST> DI void dma16(unsigned voff, u32x4 r, unsigned soff, unsigned wk) {
    asm volatile("s_add_u32 m0, %3, %4\n\ts_nop 0\n\tbuffer_load_dwordx4 %0, %1, %2 offen lds" ::"v"(voff), "s"(r), "s"(soff), "s"(wk), "i"(DST) : "memory", "scc");
}
template <int DST, int SOFF> DI void dma16i(unsigned voff, u32x4 r, unsigned wk) {
    unsigned t;
    asm volatile("s_add_u32 m0, %3, %4\n\ts_mov_b32 %0, %5\n\tbuffer_load_dwordx4 %1, %2, %0 offen lds" : "=&s"(t) : "v"(voff), "s"(r), "s"(wk), "i"(DST), "i"(SOFF) : "memory", "scc");
}
template <int DST> DI void dma16c(unsigned voff, u32x4 r, unsigned wk) {
    asm volatile("s_add_u32 m0, %2, %3\n\ts_nop 0\n\tbuffer_load_dwordx4 %0, %1, 0 offen sc1 lds" ::"v"(voff), "s"(r), "s"(wk), "i"(DST) : "memory", "scc");
}
template <int DST> DI void dma16v(unsigned voff, u32x4 r, unsigned wk) {
    asm volatile("s_add_u32 m0, %2, %3\n\ts_mov_b32 exec_lo, 0x0fffffff\n\ts_mov_b32 exec_hi, 0x0fffffff\n\tbuffer_load_dwordx4 %0, %1, 0 offen sc1 lds\n\ts_mov_b64 exec, -1" ::"v"(voff), "s"(r), "s"(wk), "i"(DST) : "memory", "scc");
}
DI void gdma4(unsigned voff, const char* sbase, unsigned ldsdst) {
    asm volatile("s_mov_b32 m0, %2\n\ts_nop 0\n\tglobal_load_lds_dword %0, %1" ::"v"(voff), "s"(sbase), "s"(ldsdst) : "memory");
}
DI bf16x8 lds128(unsigned a) { return *(const LAS bf16x8*)(size_t)a; }
DI f32x4 ldsf4(unsigned a) { return *(const LAS f32x4*)(size_t)a; }
DI float ldsf(unsigned a) { return *(const LAS float*)(size_t)a; }
DI s16x4 ldstr(unsigned a) { return __builtin_amdgcn_ds_read_tr16_b64_v4i16((LAS s16x4*)(size_t)a); }
DI unsigned pk2(float a, float b) { f32x2 p = {a, b}; return __builtin_bit_cast(unsigned, __builtin_convertvector(p, b16x2)); }
DI float rbf(float a) { return __uint_as_float(pk2(a, 0.f) << 16); }
DI float bflo(unsigned u) { return __uint_as_float(u << 16); }
DI float bfhi(unsigned u) { return __uint_as_float(u & 0xffff0000u); }
DI bf16x8 asf(u32x4 v) { return __builtin_bit_cast(bf16x8, v); }
DI float redmax4(float v) {
    auto a = __builtin_amdgcn_permlane16_swap(__float_as_uint(v), __float_as_uint(v), false, false); v = __builtin_fmaxf(__uint_as_float(a[0]), __uint_as_float(a[1]));
    auto b = __builtin_amdgcn_permlane32_swap(__float_as_uint(v), __float_as_uint(v), false, false); return __builtin_fmaxf(__uint_as_float(b[0]), __uint_as_float(b[1])); }
template <int CTRL> DI float dppadd(float v) { return v + __uint_as_float(__builtin_amdgcn_update_dpp(0, __float_as_uint(v), CTRL, 0xf, 0xf, true)); }
DI float wavesum(float v) {
    v = dppadd<0xB1>(v); v = dppadd<0x4E>(v); v = dppadd<0x141>(v); v = dppadd<0x140>(v);
    auto a = __builtin_amdgcn_permlane16_swap(__float_as_uint(v), __float_as_uint(v), false, false); v = __uint_as_float(a[0]) + __uint_as_float(a[1]);
    auto b = __builtin_amdgcn_permlane32_swap(__float_as_uint(v), __float_as_uint(v), false, false); return __uint_as_float(b[0]) + __uint_as_float(b[1]); }
DI void put_flag(rsrc_t rs, unsigned off, u32x2 v) { __builtin_amdgcn_raw_buffer_store_b64(v, rs, off, 0, 16); }
DI u32x2 get_flag(rsrc_t rs, unsigned off) { return __builtin_amdgcn_raw_buffer_load_b64(rs, off, 0, 16); }
DI unsigned last_of(unsigned cnt, unsigned n) {
    unsigned r;
    asm volatile("s_mov_b64 exec, 1\n\tds_add_rtn_u32 %0, %1, %2\n\ts_mov_b64 exec, -1\n\ts_waitcnt lgkmcnt(0)" : "=&v"(r) : "v"(cnt), "v"(1u) : "memory");
    return (unsigned)__builtin_amdgcn_readfirstlane(r) == n - 1 ? 0u : 0x80000000u;
}
DI bool all_set(u32x2 f, u32x2 ep) { return __builtin_amdgcn_ballot_w64(((f[0] ^ ep[0]) | (f[1] ^ ep[1])) != 0) == 0; }
DI void spin_flags(rsrc_t rs, unsigned off, u32x2 ep) {
#pragma nounroll
    for (unsigned it = 0; it < SPIN_MAX; ++it) {
        asm volatile("" ::: "memory");
        if (all_set(get_flag(rs, off), ep)) break;
        __builtin_amdgcn_s_sleep(SLP);
    }
    asm volatile("" ::: "memory");
}
DI void lds_arrive(unsigned a) {
    asm volatile("s_waitcnt lgkmcnt(0)\n\ts_mov_b64 exec, 1\n\tds_add_u32 %0, %1\n\ts_mov_b64 exec, -1" ::"v"(a), "v"(1u) : "memory");
}
DI void lds_wait(unsigned a, unsigned n) {
#pragma nounroll
    for (unsigned it = 0; it < SPIN_MAX; ++it) {
        asm volatile("" ::: "memory");
        if (__builtin_amdgcn_readfirstlane(__hip_atomic_load((LAS unsigned*)(size_t)a, __ATOMIC_RELAXED, __HIP_MEMORY_SCOPE_WORKGROUP)) >= n) break;
        __builtin_amdgcn_s_sleep(2);
    }
    asm volatile("" ::: "memory");
}
DI unsigned vsel(bool c, unsigned a, unsigned b) { PINV(a); PINV(b); return c ? a : b; }
DI unsigned wrap7(unsigned v) { return v >= 7 ? v - 7 : v; }
constexpr int kstep(int f, int p) {
    if (p < 5) { const int i = 5 * f + p; return 10 + 4 * (i >> 1) + (i & 1); }
    const int i = 9 * f + p - 5;
    return i < 2 ? 50 + i : (i < 12 ? i - 2 : (i < 34 ? 12 + 4 * ((i - 12) >> 1) + (i & 1) : 20 + i));
}
constexpr u64 kpack(int f, int p0) { u64 v = 0; for (int p = p0; p < p0 + 8 && p < 14; ++p) v |= (u64)kstep(f, p) << (8 * (p - p0)); return v; }
#define HOLD 1
#define WO1 5
#define WOS 4
#define PRIO_A 3
constexpr int fw(int s) { return s + 3 < 14 ? s + 3 : -1; }
constexpr int f_nw(int s) { return s == 0 ? 4 : (s == 1 ? 3 : ((s == WO1 || s == WO1 + 1) ? 4 : 0)); }
constexpr int f_np(int s) { return (s == 0 ? 1 : 0) + (s == 7 ? 1 : 0) + (s == 10 ? 1 : 0); }
constexpr int f_all(int s) { return 8 * (fw(s) >= 0) + f_nw(s) + f_np(s); }
constexpr int f_cum(int s) { int c = 16; for (int i = 0; i < s; ++i) c += f_all(i); return c; }
constexpr int f_wend(int k) { return k == 2 ? 16 : f_cum(k - 3) + 4; }
constexpr int f_hend(int k) { return k < 3 ? 4 + 4 * k : f_cum(k - 3) + 8; }
constexpr int f_wait(int s) { const int a = f_cum(s) - f_hend(s), b = (s >= 1 && s <= 12) ? f_cum(s) - f_wend(s + 1) : 63; return a < b ? a : b; }

#define STAMP(i) do { } while (0)
#define FIN() do { } while (0)
#define DIAGPAR
#define DIAGARG

struct HalfFrag { s16x4 lo, hi; };
DI bf16x8 join(const HalfFrag& h) { return __builtin_shufflevector(h.lo, h.hi, 0, 1, 2, 3, 4, 5, 6, 7); }
DI void rd_tr(HalfFrag& h, unsigned base, unsigned c, unsigned d) {
    if (c + d < 65536) { h.lo = ldstr(base + c); h.hi = ldstr(base + c + d); }
    else { unsigned a = base + c; PINV(a); h.lo = ldstr(a); h.hi = ldstr(a + d); }
}

struct P1 {
    unsigned xa[2], wa[2][2];
    u32x4 rx, rq, r1, rw;
    unsigned xv0, xv1, v0, v1, v2;
    unsigned m1, wk, c0;
    rsrc_t rs; unsigned gb, gs, tag, lane;
    u32x4 srow; float sv[8], sm, sq;
};
template <int N> DI void piece(const P1& P) {
    if constexpr (N < 28) {
        const unsigned kc = wrap7(P.c0 + (N >> 2)) * 128;
        if constexpr ((N & 3) == 0) dma16<phys(N)>(P.xv0, P.rx, kc, P.wk);
        else if constexpr ((N & 3) == 1) dma16<phys(N)>(P.xv1, P.rx, kc, P.wk);
        else if constexpr ((N & 3) == 2) dma16<phys(N)>(P.v0, P.rq, kc * 1344, P.wk);
        else dma16<phys(N)>(P.v1, P.r1, kc * P.m1, P.wk);
    } else if constexpr (N < 35) {
        dma16<phys(N)>(P.v2, P.rw, wrap7(P.c0 + (N - 28)) * (128 * 1792), P.wk);
    }
}
template <int S> DI void rd_ax(bf16x8 (&xf)[14][2], int tt, const P1& P) { if constexpr (S < 14) xf[S][tt] = lds128(P.xa[S & 1] + phys(4 * (S >> 1)) + tt * 2048); }
template <int S> DI void rd_aw(HalfFrag (&wf)[2][2][2], int b, int nt, const P1& P) { if constexpr (S < 14) rd_tr(wf[S & 1][b][nt], P.wa[S & 1][nt], phys(4 * (S >> 1) + 2 + b), 512); }
constexpr int a_first(int s) { return s == 0 ? 8 : (s == 1 ? 12 : (s == 2 ? 16 : 14 + 2 * s)); }
constexpr int a_count(int s) { return s <= 2 ? 4 : 2; }
DI u32x2 ld_gran(const P1& P) { unsigned ln = threadIdx.x; PINV(ln); return __builtin_amdgcn_raw_buffer_load_b64(P.rs, P.gb + (ln & 31) * 8, 0, 16); }
template <int S> DI void stats_stage(P1& P) {
    if constexpr (S == 0) {
#pragma unroll
        for (int j = 0; j < 4; ++j) { const unsigned u = P.srow[j]; P.sv[2 * j] = bflo(u); P.sv[2 * j + 1] = bfhi(u); }
        P.sm = ((P.sv[0] + P.sv[1]) + (P.sv[2] + P.sv[3])) + ((P.sv[4] + P.sv[5]) + (P.sv[6] + P.sv[7])); PINV(P.sm);
    } else if constexpr (S == 1) { P.sm = wavesum(P.sm) * (1.f / 448.f); PINV(P.sm);
    } else if constexpr (S == 2) {
        float q0 = 0.f, q1 = 0.f;
#pragma unroll
        for (int j = 0; j < 4; ++j) { const float d0 = P.sv[j] - P.sm, d1 = P.sv[j + 4] - P.sm; q0 = __builtin_fmaf(d0, d0, q0); q1 = __builtin_fmaf(d1, d1, q1); }
        P.sq = P.lane < 56 ? q0 + q1 : 0.f; PINV(P.sq);
    } else if constexpr (S == 3) { P.sq = wavesum(P.sq) * (1.f / 448.f); PINV(P.sq);
    } else if constexpr (S == 4) {
        const unsigned sdm = pk2(__builtin_amdgcn_sqrtf(rbf(rbf(P.sq) + 1e-5f)), P.sm);
        put_flag(P.rs, P.gs + (P.lane == 0 ? 0 : 0x80000000u), (u32x2){sdm, P.tag});
    }
}
struct Epi { float mr[2], rstd[2], csq[2][2]; f32x4 mq[2], rq[2]; unsigned csw; int s1; };
DI void gran_check(u32x2& R, const P1& P) {
#pragma nounroll
    for (unsigned it = 0; it < SPIN_MAX; ++it) {
        if (__builtin_amdgcn_ballot_w64(R[1] != P.tag) == 0) break;
        __builtin_amdgcn_s_sleep(1);
        asm volatile("" ::: "memory");
        R = ld_gran(P);
    }
}
template <bool B1Q, int S>
DI void step_a(bf16x8 (&xf)[14][2], HalfFrag (&wf)[2][2][2], f32x4 (&acc)[2][2][2], f32x4 (&cs)[2], bf16x8 ones, P1& P, u32x2& R, Epi& E DIAGPAR) {
    if constexpr (S & 1) {
        constexpr int c = (S + 1) >> 1;
        if constexpr (c == 1) WAITBAR(5); else if constexpr (c == 2) WAITBAR(8); else if constexpr (c < 6) WAITBAR(9); else if constexpr (c == 6) WAITBAR(8); else WAITBAR(0);
        STAMP(16 + c);
    }
    constexpr int f = a_first(S), n = a_count(S);
    float ca[2][2], cb_[2][2];
    if constexpr (S == 12) { cs[0] = MFMA(ones, join(wf[0][0][0]), cs[0]); cs[1] = MFMA(ones, join(wf[0][1][0]), cs[1]); SB(); }
    if constexpr (S == 13) {
        const unsigned st = L_ST + (P.wk >> 2);
        unsigned ln = threadIdx.x; PINV(ln);
#pragma unroll
        for (int tt = 0; tt < 2; ++tt) {
            E.rq[tt] = ldsf4(st + tt * 64 + (ln & 48)); E.mq[tt] = ldsf4(st + 128 + tt * 64 + (ln & 48));
            E.rstd[tt] = ldsf(st + tt * 64 + (ln & 15) * 4); E.mr[tt] = ldsf(st + 128 + tt * 64 + (ln & 15) * 4);
        }
#pragma unroll
        for (int i = 0; i < 4; ++i) { const unsigned a = (E.csw & ~(256u | 64u)) + (i >> 1) * 512 + ((i & 1) ^ E.s1) * 64; ca[i >> 1][i & 1] = ldsf(a); cb_[i >> 1][i & 1] = ldsf(a + 256); }
        SB();
    }
#define MA(b, nt, tt) do { acc[b][nt][tt] = (b == 0 || B1Q) ? MFMA(xf[S][tt], join(wf[S & 1][b][nt]), acc[b][nt][tt]) : MFMA(join(wf[S & 1][b][nt]), xf[S][tt], acc[b][nt][tt]); } while (0)
    MA(0, 0, 0); rd_ax<S + 1>(xf, 0, P); SB();
    MA(0, 0, 1); rd_aw<S + 1>(wf, 0, 0, P); SB();
    MA(0, 1, 0); piece<f>(P); SB();
    MA(0, 1, 1); rd_ax<S + 1>(xf, 1, P); SB();
    if constexpr (n > 2) { piece<f + 2>(P); SB(); }
    MA(1, 0, 0); rd_aw<S + 1>(wf, 0, 1, P); SB();
    MA(1, 0, 1); piece<f + 1>(P); SB();
    if constexpr (S == 12) {
        unsigned t1 = threadIdx.x; PINV(t1);
        E.csw += (t1 & 15) * 4;
        *(LAS float*)(size_t)(E.csw) = cs[0][0]; *(LAS float*)(size_t)(E.csw + 512) = cs[1][0]; SB();
        gran_check(R, P);
        const float rs_ = __builtin_amdgcn_rcpf(bflo(R[0])); const unsigned st = L_ST + (P.wk >> 2) + (t1 & 31) * 4;
        *(LAS float*)(size_t)(st) = rs_; *(LAS float*)(size_t)(st + 128) = bfhi(R[0]) * rs_; SB();
    }
    if constexpr (S == 13) {
#pragma unroll
        for (int i = 0; i < 4; ++i) { E.csq[i >> 1][i & 1] = ca[i >> 1][i & 1] + cb_[i >> 1][i & 1]; PINV(E.csq[i >> 1][i & 1]); }
        SB();
    }
    MA(1, 1, 0); rd_aw<S + 1>(wf, 1, 0, P); SB();
    MA(1, 1, 1); rd_aw<S + 1>(wf, 1, 1, P); SB();
#undef MA
    if constexpr ((S & 1) == 0 && S < 12) { cs[0] = MFMA(ones, join(wf[0][0][0]), cs[0]); cs[1] = MFMA(ones, join(wf[0][1][0]), cs[1]); SB(); }
    if constexpr (n > 2) { piece<f + 3>(P); SB(); }
    if constexpr (S <= 4) { stats_stage<S>(P); SB(); }
    if constexpr (S == 10) { R = ld_gran(P); SB(); }
    if constexpr (S < 13) step_a<B1Q, S + 1>(xf, wf, acc, cs, ones, P, R, E DIAGARG);
}
template <bool B1Q>
DI void pass_a(bf16x8 (&xf)[14][2], f32x4 (&acc)[2][2][2], bf16x8 ones, P1& P, Epi& E DIAGPAR) {
    HalfFrag wf[2][2][2]; u32x2 R; f32x4 cs[2] = {ZERO4, ZERO4};
    STAMP(13);
    rd_ax<0>(xf, 0, P); rd_aw<0>(wf, 0, 0, P); rd_ax<0>(xf, 1, P); rd_aw<0>(wf, 0, 1, P); rd_aw<0>(wf, 1, 0, P); rd_aw<0>(wf, 1, 1, P); SB();
    step_a<B1Q, 0>(xf, wf, acc, cs, ones, P, R, E DIAGARG);
}
DI unsigned qkv_dest(unsigned n, unsigned tb, unsigned g) {
    const unsigned hs = (n * 2341u) >> 16, d = n - hs * 28u;
    const unsigned isv = hs >> 5, pl = 448 + 64 * isv;
    unsigned o = WS_Q - isv * (32 * 64 * 256) + (hs * 256 + tb * 4 + g) * pl + d * 16 + ((d >> 3) & 1 & ~isv);
    PINV(o); return o;
}
DI void epi_qkv(const f32x4 (&acc)[2][2], const f32x4 (&mr)[2], const f32x4 (&rq)[2], const float (&cs)[2], const unsigned (&q)[2], rsrc_t rs) {
#pragma unroll
    for (int nt = 0; nt < 2; ++nt) {
        unsigned w[2][2];
#pragma unroll
        for (int tt = 0; tt < 2; ++tt) {
            const f32x4 v = acc[nt][tt] * rq[tt] - mr[tt] * cs[nt];
            w[tt][0] = pk2(v[0], v[1]); w[tt][1] = pk2(v[2], v[3]);
        }
        const bool sw = q[nt] & 1;
        const u32x4 o = {sw ? w[1][0] : w[0][0], sw ? w[1][1] : w[0][1], sw ? w[0][0] : w[1][0], sw ? w[0][1] : w[1][1]};
        __builtin_amdgcn_raw_buffer_store_b128(o, rs, q[nt] & ~15u, 0, 16);
    }
}
DI void epi_ffn(const f32x4 (&acc)[2][2], u32x4 (&o)[2], const unsigned (&ta)[2], int csb, int b1i, bool s1, const float (&mr)[2], const float (&rstd)[2]) {
    f32x4 cs[2], bb[2];
#pragma unroll
    for (int nt = 0; nt < 2; ++nt) {
        cs[nt] = ldsf4(csb + ta[nt] * 2) + ldsf4(csb + 256 + ta[nt] * 2);
        const u32x2 b = *(const LAS u32x2*)(size_t)(L_B1 + b1i * 256 + ta[nt]);
        bb[nt] = (f32x4){bflo(b[0]), bfhi(b[0]), bflo(b[1]), bfhi(b[1])};
    }
#pragma unroll
    for (int tt = 0; tt < 2; ++tt) {
        unsigned w[2][2];
#pragma unroll
        for (int nt = 0; nt < 2; ++nt) {
            f32x4 u = acc[nt][tt] * rstd[tt] + (bb[nt] - mr[tt] * cs[nt]);
            u = __builtin_elementwise_max(u, ZERO4); u = u * u;
            w[nt][0] = pk2(u[0], u[1]); w[nt][1] = pk2(u[2], u[3]);
        }
        o[tt] = s1 ? (u32x4){w[1][0], w[1][1], w[0][0], w[0][1]} : (u32x4){w[0][0], w[0][1], w[1][0], w[1][1]};
    }
}
struct PB { u32x4 rws; rsrc_t rs; u32x2 ep; unsigned f0, f1, qpo; u32x2 pl[2]; bool two; };
DI void poll_asm(u32x2& d, unsigned off, u32x4 r) { asm volatile("buffer_load_dwordx2 %0, %1, %2, 0 offen sc1" : "=v"(d) : "v"(off), "s"(r) : "memory"); }
DI void flag_both(const PB& B) { const unsigned lo = last_of(L_CNT + 16, 8); put_flag(B.rs, B.f0 | lo, B.ep); put_flag(B.rs, B.f1 | lo, B.ep); }
template <bool X27, int S>
DI void step_x(const bf16x8 (&xf)[14][2], HalfFrag (&wf)[3][4], f32x4 (&ac)[4][2], f32x4 (&cs)[4], bf16x8 ones, const unsigned (&wa)[2][4], PB& B) {
    if constexpr (X27 && S == 7) { WAITV(0); lds_arrive(L_CNT + 40); lds_wait(L_CNT + 40, 4); }
#pragma unroll
    for (int nt = 0; nt < 4; ++nt) {
        ac[nt][0] = MFMA(join(wf[S % 3][nt]), xf[S][0], S == 0 ? ZERO4 : ac[nt][0]);
        if constexpr (S < 12) rd_tr(wf[(S + 2) % 3][nt], wa[S & 1][nt], ((S + 2) >> 1) * UNIT, 512);
        SB();
        ac[nt][1] = MFMA(join(wf[S % 3][nt]), xf[S][1], S == 0 ? ZERO4 : ac[nt][1]);
        if constexpr (X27) cs[nt] = MFMA(join(wf[S % 3][nt]), ones, S == 0 ? ZERO4 : cs[nt]);
        SB();
    }
    if constexpr (!X27) {
        if constexpr (S == FQ_AT) { WAITV(0); flag_both(B); SB(); }
        if constexpr (S == 13) { poll_asm(B.pl[1], B.qpo, B.rws); B.pl[0] = B.ep; SB(); }
    }
    if constexpr (S < 13) step_x<X27, S + 1>(xf, wf, ac, cs, ones, wa, B);
}
DI void pre_x(HalfFrag (&wf)[3][4], unsigned (&wa)[2][4], const P1& P, unsigned c32, unsigned base) {
#pragma unroll
    for (int i = 0; i < 8; ++i) { wa[i >> 2][i & 3] = (P.wa[i >> 2][0] ^ ((c32 ^ (i & 3)) << 5)) + base; PINV(wa[i >> 2][i & 3]); }
#pragma unroll
    for (int i = 0; i < 8; ++i) rd_tr(wf[i >> 2][i & 3], wa[i >> 2][i & 3], 0, 512);
    SB();
}
DI void epi_x(const f32x4 (&ac)[4][2], const f32x4 (&cs)[4], unsigned b1a, const Epi& E, rsrc_t rs, unsigned dst, int c0, int c1) {
#pragma unroll
    for (int c = c0; c < c1; ++c) {
        f32x4 bb[2];
#pragma unroll
        for (int n = 0; n < 2; ++n) { const u32x2 b = *(const LAS u32x2*)(size_t)(b1a + c * 64 + n * 32); bb[n] = (f32x4){bflo(b[0]), bfhi(b[0]), bflo(b[1]), bfhi(b[1])}; }
#pragma unroll
        for (int tt = 0; tt < 2; ++tt) {
            f32x4 u0 = ac[2 * c][tt] * E.rstd[tt] + (bb[0] - E.mr[tt] * cs[2 * c]), u1 = ac[2 * c + 1][tt] * E.rstd[tt] + (bb[1] - E.mr[tt] * cs[2 * c + 1]);
            u0 = __builtin_elementwise_max(u0, ZERO4); u0 = u0 * u0; u1 = __builtin_elementwise_max(u1, ZERO4); u1 = u1 * u1;
            __builtin_amdgcn_raw_buffer_store_b128((u32x4){pk2(u0[0], u0[1]), pk2(u0[2], u0[3]), pk2(u1[0], u1[1]), pk2(u1[2], u1[3])}, rs, dst + c * 1024 + tt * 56 * 1024, 0, 16);
        }
    }
}

struct PF { u32x4 rw2, rwo, rws; rsrc_t rs; u32x2 ep, fl, fl2; u64 t0, t1; unsigned v[4], hv[4], vo, ring, flo, flo2; const char* xp; u32x4 xres; unsigned wb[4]; };
DI unsigned f_ks(const PF& F, int p) { return (unsigned)(p < 8 ? F.t0 >> (8 * p) : F.t1 >> (8 * (p - 8))) & 63u; }
template <int KS> DI void f_ldh(u32x4 (&hf)[14][4], int tt, const PF& F) {
    if constexpr (KS >= 0) { asm volatile("buffer_load_dwordx4 %0, %1, %2, %3 offen sc1" : "=v"(hf[KS][tt]) : "v"(F.hv[tt]), "s"(F.rws), "s"(f_ks(F, KS) * 1024) : "memory"); SB(); }
}
template <int KS> DI void f_rdw(HalfFrag (&wf)[2][4], int nt, const PF& F) { if constexpr (KS < 14) rd_tr(wf[KS & 1][nt], F.wb[nt], rslot(KS), 2048); }
template <int KS, int I> DI void f_dma(const PF& F) { if constexpr (KS >= 0) { dma16<L_RING + rslot(KS) + I * 1024>(F.v[I], F.rw2, f_ks(F, KS) * (32 * 896), F.ring); SB(); } }
template <int S, int I> DI void f_wod(const PF& F) {
    if constexpr (I < f_nw(S)) {
        if constexpr (S == WO1 && I == 0) lds_wait(L_CNT + 4, 4);
        if constexpr (S < 2) dma16i<L_WO2 + (4 * S + I) * 4096, (252 + (4 * S + I) * 32) * 896>(F.vo, F.rwo, F.ring >> 3);
        else dma16i<L_WO + (4 * (S - WO1) + I) * 4096, (4 * (S - WO1) + I) * 32 * 896>(F.vo, F.rwo, F.ring >> 3);
        SB();
    }
}
template <int S>
DI void step_f(u32x4 (&hf)[14][4], HalfFrag (&wf)[2][4], f32x4 (&ac)[4][4], PF& F DIAGPAR) {
    STAMP(32 + S);
    asm volatile("s_waitcnt vmcnt(%4)" : "+v"(hf[S][0]), "+v"(hf[S][1]), "+v"(hf[S][2]), "+v"(hf[S][3]) : "i"(f_wait(S)) : "memory");
    if constexpr (S == WO1 + WOS) { WAITV(f_cum(S) - (f_cum(WO1 + 2) - f_np(WO1 + 1))); lds_arrive(L_CNT + 8); }
#define MF(tt, nt) do { ac[tt][nt] = MFMA(join(wf[S & 1][nt]), asf(hf[S][tt]), S == 0 ? ZERO4 : ac[tt][nt]); } while (0)
    MF(0, 0); f_dma<fw(S), 0>(F); MF(0, 1); SB(); f_dma<fw(S), 1>(F); MF(0, 2); SB(); f_dma<fw(S), 2>(F); MF(0, 3); SB(); f_dma<fw(S), 3>(F);
    MF(1, 0); f_rdw<S + 1>(wf, 0, F); SB(); MF(1, 1); f_rdw<S + 1>(wf, 1, F); SB();
    MF(1, 2); f_rdw<S + 1>(wf, 2, F); SB(); MF(1, 3); f_rdw<S + 1>(wf, 3, F); SB();
    if constexpr (S == 2) { asm volatile("s_waitcnt vmcnt(%1)" : "+v"(F.fl) : "i"(f_all(1) + 4) : "memory"); if (!all_set(F.fl, F.ep)) spin_flags(F.rs, F.flo, F.ep); }
    if constexpr (S == 9) { asm volatile("s_waitcnt vmcnt(%1)" : "+v"(F.fl2) : "i"(f_all(8) + 4) : "memory"); if (!all_set(F.fl2, F.ep)) spin_flags(F.rs, F.flo2, F.ep); }
    f_ldh<fw(S)>(hf, 0, F); MF(2, 0); SB(); f_ldh<fw(S)>(hf, 1, F); MF(2, 1); SB(); f_ldh<fw(S)>(hf, 2, F); MF(2, 2); SB(); f_ldh<fw(S)>(hf, 3, F); MF(2, 3); SB();
    f_wod<S, 0>(F); MF(3, 0); SB(); f_wod<S, 1>(F); MF(3, 1); SB(); f_wod<S, 2>(F); MF(3, 2); SB(); f_wod<S, 3>(F); MF(3, 3); SB();
#undef MF
    if constexpr (S == 0) { poll_asm(F.fl, F.flo, F.rws); SB(); }
    if constexpr (S == 7) { poll_asm(F.fl2, F.flo2, F.rws); SB(); }
    if constexpr (S == 10) { F.xres = *(const GAS u32x4*)F.xp; SB(); }
    if constexpr (S < 13) step_f<S + 1>(hf, wf, ac, F DIAGARG);
}

struct Rest { const char *x, *wo, *w2; char *out, *ws; unsigned wv, hd, art, rt64, cb, fb2, s1, hst, fhw, l0; bool extra, last; };
template <bool SPARE>
DI void rest(const bf16x8 (&xf)[14][2], u32x4 ones4, const P1& P, PB& B, const Epi& E, const Rest& A DIAGPAR) {
    const bf16x8 ones = asf(ones4); const rsrc_t rs = B.rs; const u32x2 ep = B.ep; const unsigned wv = A.wv, hd = A.hd, art = A.art, rt64 = A.rt64, w4 = wv & 3;
    unsigned tid2 = threadIdx.x; PINV(tid2);
    const unsigned lane2 = tid2 & 63, r16b = lane2 & 15, gb = lane2 >> 4, q4b = r16b >> 2, p4b = lane2 & 3;
    u32x4 xres;
    if (wv >= 4) {
        STAMP(24);
        WAITV(0);
        flag_both(B);
        __builtin_amdgcn_s_setprio(0);
        STAMP(25);
#define COLSUM2() do {     \
            unsigned a[2]; \
_Pragma("unroll") \
            for (int e = 0; e < 2; ++e) { a[e] = (P.wa[e][0] ^ (((2 + A.s1) ^ w4) << 5)) + L_REG; PINV(a[e]); } \
            HalfFrag cf[14]; f32x4 c2 = ZERO4; \
_Pragma("unroll") \
            for (int i = 0; i < 14; ++i) rd_tr(cf[i], a[i & 1], (i >> 1) * UNIT, 512); \
_Pragma("unroll") \
            for (int i = 0; i < 14; ++i) c2 = MFMA(ones, join(cf[i]), c2); \
            *(LAS float*)(size_t)(L_CS2 + (w4 * 16 + r16b) * 4) = c2[0]; \
            lds_arrive(L_CNT + 48); \
        } while (0)
        if constexpr (!SPARE) {
            const unsigned oxo = (rt64 * 64 + (tid2 >> 3)) * 896 + A.cb * 128 + (tid2 & 7) * 16;
            PF F; F.rw2 = mkrsrc(A.w2, 1792 * 896); F.rwo = mkrsrc(A.wo, 448 * 896); F.rws = B.rws; F.rs = rs; F.ep = ep; F.ring = w4 * UNIT; F.xp = A.x + oxo;
            F.t0 = w4 == 0 ? kpack(0, 0) : (w4 == 1 ? kpack(1, 0) : (w4 == 2 ? kpack(2, 0) : kpack(3, 0)));
            F.t1 = w4 == 0 ? kpack(0, 8) : (w4 == 1 ? kpack(1, 8) : (w4 == 2 ? kpack(2, 8) : kpack(3, 8)));
#pragma unroll
            for (int i = 0; i < 4; ++i) {
                F.hv[i] = WS_H + (4 * rt64 + i) * (56 * 1024) + lane2 * 16;
                F.v[i] = ((lane2 >> 3) + 8 * i) * 896 + (((lane2 ^ (lane2 >> 3 & 6)) & 7) << 4) + A.cb * 128;
            }
            f_dma<0, 0>(F); f_dma<0, 1>(F); f_dma<0, 2>(F); f_dma<0, 3>(F); f_dma<1, 0>(F); f_dma<1, 1>(F); f_dma<1, 2>(F); f_dma<1, 3>(F);
            COLSUM2();
            F.vo = F.v[0] + w4 * 7168;
            const unsigned fhb = WS_FH + (rt64 >> 1) * (28 * 128), p7 = 5 + min(lane2, 6u);
            const unsigned fle = fhb + (((unsigned)(F.t0 >> (8 * min(lane2, 4u))) & 63u) >> 1) * 128;
            F.flo = fhb + (((unsigned)(p7 < 8 ? F.t0 >> (8 * p7) : F.t1 >> (8 * (p7 - 8))) & 63u) >> 1) * 128;
            F.flo2 = fhb + (((unsigned)(F.t1 >> (32 + 8 * (lane2 & 1))) & 63u) >> 1) * 128;
            const unsigned key3 = (2 * gb + (q4b >> 1)) & 3;
#pragma unroll
            for (unsigned nt = 0; nt < 4; ++nt) { F.wb[nt] = L_RING + F.ring + (4 * gb + q4b) * 128 + ((nt ^ key3) << 5) + p4b * 8; PINV(F.wb[nt]); }
            PINV(F.flo); PINV(F.flo2);
            __builtin_amdgcn_s_sleep(ESLP);
            spin_flags(rs, fle, ep);
            if (HOLD) lds_wait(L_CNT + 60, 4);
            STAMP(29);
            u32x4 hf[14][4];
#pragma unroll
            for (int i = 0; i < 4; ++i) f_ldh<0>(hf, i, F);
#pragma unroll
            for (int i = 0; i < 4; ++i) f_ldh<1>(hf, i, F);
#pragma unroll
            for (int i = 0; i < 4; ++i) f_ldh<2>(hf, i, F);
            HalfFrag wf[2][4]; f32x4 ac[4][4];
#pragma unroll
            for (int nt = 0; nt < 4; ++nt) f_rdw<0>(wf, nt, F);
            lds_wait(L_CNT + 52, 4);
            f_dma<2, 0>(F); f_dma<2, 1>(F); f_dma<2, 2>(F); f_dma<2, 3>(F);
            STAMP(5);
            step_f<0>(hf, wf, ac, F DIAGARG);
            STAMP(8);
#pragma unroll
            for (int i = 0; i < 16; ++i) *(LAS f32x4*)(size_t)(L_RING + F.ring + (i >> 3) * 4 * UNIT + ((i >> 2) & 1) * 4096 + r16b * 256 + ((((i & 3) * 4 + gb) ^ r16b) << 4)) = ac[i >> 2][i & 3];
            xres = F.xres;
            BAR();
        } else {
            const unsigned v1 = P.v2 + (27 - A.fb2) * 128, v0 = v1 - 32 * 3584;
#define X27(i) do { const unsigned so = wrap7(P.c0 + i) * (128 * 1792); dma16<L_RING + i * UNIT - 4096>(v0, P.rw, so, P.wk); dma16<L_RING + i * UNIT>(v1, P.rw, so, P.wk); } while (0)
            X27(0); X27(1); X27(2); X27(3); X27(4);
            COLSUM2();
            if (A.extra) {
                WAITV(0); lds_arrive(L_CNT + 56); lds_wait(L_CNT + 56, 4); lds_wait(L_CNT + 52, 4);
                X27(5); X27(6);
                unsigned wa[2][4]; HalfFrag wf[3][4]; f32x4 ac[4][2], cs[4];
                pre_x(wf, wa, P, 2 + A.s1, L_RING);
                step_x<true, 0>(xf, wf, ac, cs, ones, wa, B);
                epi_x(ac, cs, L_B1 + gb * 8, E, rs, A.hst + 27 * 2048, 0, 2);
                WAITV(0);
                put_flag(rs, (A.fhw + 27 * 128) | last_of(L_CNT + 28, 4), ep);
            }
#undef X27
            FIN();
            __builtin_amdgcn_endpgm();
        }
    } else {
        const unsigned oxo = (rt64 * 64 + (tid2 >> 3)) * 896 + A.cb * 128 + (tid2 & 7) * 16, fbo = A.fhw + A.fb2 * 128;
        {
            unsigned wa[2][4]; HalfFrag wf[3][4]; f32x4 ac[4][2], cs[4];
            pre_x(wf, wa, P, A.s1, L_REG);
            {
                const unsigned ln = min(lane2, 13u), sel = ln >= 7, m = ln - 7 * sel, sec = (m >= 1) + (m >= 4);
                const int t = (int)(art + m) - 3 * (int)sec;
                B.qpo = WS_FQ + ((t < 0 ? art : t) * 21 + ((448 * sec + 28 * hd + 27 * sel) >> 6)) * 128 + (wv == 0 ? 0 : OOB);
                PINV(B.qpo);
            }
            STAMP(24);
            step_x<false, 0>(xf, wf, ac, cs, ones, wa, B);
            lds_arrive(L_CNT + 52);
            STAMP(3);
            const u32x4 rQ = mkrsrc(A.ws + WS_Q + hd * (64 * 1792), (4 * art + 4) * 1792), rK = mkrsrc(A.ws + WS_K + hd * (64 * 1792), (4 * art + 4) * 1792), rV = mkrsrc(A.ws + WS_V + hd * (64 * 2048), 64 * 2048);
            unsigned kv0 = tid2 * 16 + (4 * art - 8) * 1792, vv0 = tid2 * 16 + (4 * art - 8) * 2048;
            PINV(kv0); PINV(vv0);
            *(LAS u32x4*)(size_t)(L_V + min(lane2, 11u) * 2048 + 28 * 16) = ones4;
            __builtin_amdgcn_s_setprio(PRIO_A);
            lds_wait(L_CNT + 48, 4);
#pragma unroll
            for (int nt = 0; nt < 4; ++nt) cs[nt] = ldsf4(L_CS2 + nt * 64 + gb * 16);
            epi_x(ac, cs, L_B1 + 256 + gb * 8, E, rs, A.hst + A.fb2 * 2048, 0, 1);
            asm volatile("s_waitcnt vmcnt(2)" : "+v"(B.pl[0]), "+v"(B.pl[1])::"memory");
            if (wv == 0) { if (!all_set(B.pl[1], ep)) spin_flags(rs, B.qpo, ep); lds_arrive(L_CNT + 36); } else lds_wait(L_CNT + 36, 1);
            STAMP(28);
            dma16c<L_Q>(kv0 + 8 * 1792, rQ, P.wk); dma16c<L_Q + 4096>(kv0 + 8 * 1792 + 4096, rQ, P.wk);
            dma16c<L_K>(kv0, rK, P.wk); dma16c<L_K + 4096>(kv0 + 4096, rK, P.wk); dma16c<L_K + 2 * 4096>(kv0 + 2 * 4096, rK, P.wk);
            dma16c<L_K + 3 * 4096>(kv0 + 3 * 4096, rK, P.wk); dma16c<L_K + 4 * 4096>(kv0 + 4 * 4096, rK, P.wk); dma16c<L_K + 5 * 4096>(kv0 + 5 * 4096, rK, P.wk);
            lds_arrive(L_CNT + 60);
            epi_x(ac, cs, L_B1 + 256 + gb * 8, E, rs, A.hst + A.fb2 * 2048, 1, 2);
            dma16v<L_V>(vv0, rV, P.wk); dma16v<L_V + 4096>(vv0 + 4096, rV, P.wk); dma16v<L_V + 2 * 4096>(vv0 + 2 * 4096, rV, P.wk);
            dma16v<L_V + 3 * 4096>(vv0 + 3 * 4096, rV, P.wk); dma16v<L_V + 4 * 4096>(vv0 + 4 * 4096, rV, P.wk); dma16v<L_V + 5 * 4096>(vv0 + 5 * 4096, rV, P.wk);
            STAMP(29);
        }
        unsigned ka0 = L_K + wv * 1792 + p4b * 448 + (8 * gb + q4b) * 16 + ((gb & 1) << 3), ka1 = ka0 ^ 8;
        unsigned va0 = L_V + wv * 2048 + gb * 512 + r16b * 16, va1 = L_V + wv * 2048 + vsel(p4b == 3, 28 * 16, gb * 512 + (16 + 3 * q4b + p4b) * 16);
        unsigned ost = WS_O + ((8 * art + 2 * wv) * 16 + hd) * 1024 + lane2 * 16;
        f32x4 cml, cmh;
#pragma unroll
        for (int j = 0; j < 4; ++j) { const bool newer = 4 * gb + j > r16b; cml[j] = newer ? 0.f : -INFINITY; cmh[j] = newer ? -INFINITY : 0.f; }
        PINV(ka0); PINV(ka1); PINV(va0); PINV(va1); PINV(ost); PINV(cml); PINV(cmh);
        WAITV(8); lds_arrive(L_CNT); lds_wait(L_CNT, 4);
        STAMP(5);
        {
            HalfFrag qh[2], kf[18];
            rd_tr(qh[0], ka0 - (L_K - L_Q), 0, 64); rd_tr(qh[1], ka1 - (L_K - L_Q), 0, 64);
            f32x4 s[2][18];
            bf16x8 vf[9][2];
            float m[2] = {-INFINITY, -INFINITY};
#define MX(u, t) do { m[u] = __builtin_fmaxf(__builtin_fmaxf(m[u], s[u][t][0]), s[u][t][1]); m[u] = __builtin_fmaxf(__builtin_fmaxf(m[u], s[u][t][2]), s[u][t][3]); PINV(m[u]); } while (0)
            if (art >= 2) {
#pragma unroll
                for (int t = 0; t < 3; ++t) rd_tr(kf[t], (t & 1) ? ka1 : ka0, (t >> 1) * 1792, 64);
                if (gb == 3) { qh[0].hi = (s16x4){0, 0, 0, 0}; qh[1].hi = (s16x4){0, 0, 0, 0}; }
                const bf16x8 qf0 = join(qh[0]), qf1 = join(qh[1]);
#pragma unroll
                for (int t = 0; t < 18; ++t) {
                    if (t < 17) s[0][t] = MFMA(join(kf[t]), qf0, t == 0 ? cml : (t == 16 ? cmh : ZERO4));
                    if (t + 3 < 18) rd_tr(kf[t + 3], (t & 1) ? ka0 : ka1, ((t + 3) >> 1) * 1792, 64);
                    if (t >= 2) MX(0, t - 2);
                    SB();
                    if (t > 0) s[1][t] = MFMA(join(kf[t]), qf1, t == 1 ? cml : (t == 17 ? cmh : ZERO4));
                    if (t >= 3) MX(1, t - 2);
                    if (t == 2) { WAITV(6); put_flag(rs, fbo | last_of(L_CNT + 24, 4), ep); }
                    if (t == 14) { WAITV(1); lds_arrive(L_CNT + 44); }
                    SB();
                }
                MX(0, 16); MX(1, 16); MX(1, 17);
            } else {
                const int b0 = 8 - 4 * (int)art - (int)wv;
                WAITV(0); put_flag(rs, fbo | last_of(L_CNT + 24, 4), ep); lds_arrive(L_CNT + 44);
                if (gb == 3) { qh[0].hi = (s16x4){0, 0, 0, 0}; qh[1].hi = (s16x4){0, 0, 0, 0}; }
                const bf16x8 qf0 = join(qh[0]), qf1 = join(qh[1]);
                const f32x4 ninf = {-INFINITY, -INFINITY, -INFINITY, -INFINITY};
#pragma unroll
                for (int b = 0; b < 9; ++b) {
                    if (b >= b0) {
                        rd_tr(kf[0], ka0, b * 1792, 64); rd_tr(kf[1], ka1, b * 1792, 64);
                        s[0][2 * b] = MFMA(join(kf[0]), qf0, b == 8 ? cmh : ZERO4); s[1][2 * b] = MFMA(join(kf[0]), qf1, ZERO4);
                        s[0][2 * b + 1] = MFMA(join(kf[1]), qf0, ZERO4); s[1][2 * b + 1] = MFMA(join(kf[1]), qf1, b == 8 ? cmh : ZERO4);
                        MX(0, 2 * b); MX(1, 2 * b); if (b < 8) MX(0, 2 * b + 1); MX(1, 2 * b + 1);
                    } else { s[0][2 * b] = ninf; s[1][2 * b] = ninf; s[0][2 * b + 1] = ninf; s[1][2 * b + 1] = ninf; }
                }
            }
#undef MX
            if constexpr (!SPARE) lds_arrive(L_CNT + 4);
            const float SC = 0.27264391f;
            const float mc[2] = {-SC * redmax4(m[0]), -SC * redmax4(m[1])};
            lds_wait(L_CNT + 44, 4);
#pragma unroll
            for (int b = 0; b < 2; ++b) { vf[b][0] = lds128(va0 + b * 2048); vf[b][1] = lds128(va1 + b * 2048); }
            STAMP(6);
            f32x4 o[2][2] = {{ZERO4, ZERO4}, {ZERO4, ZERO4}};
            u32x4 pw[2];
#pragma unroll
            for (int i = 0; i < 19; ++i) {
                const int b = i >> 1, u = i & 1, pb = (i - 1) >> 1, pu = (i - 1) & 1;
                const bool lo = i < 18 && (u == 0 || b > 0), hi = i < 18 && (u == 1 || b < 8);
                f32x4 ea = ZERO4, eb = ZERO4;
                if (lo) ea = s[u][2 * b] * SC + mc[u];
                if (hi) eb = s[u][2 * b + 1] * SC + mc[u];
                if (u == 0 && b + 2 < 9) { vf[b + 2][0] = lds128(va0 + (b + 2) * 2048); vf[b + 2][1] = lds128(va1 + (b + 2) * 2048); }
                SB();
                if (lo) { ea[0] = __builtin_amdgcn_exp2f(ea[0]); ea[1] = __builtin_amdgcn_exp2f(ea[1]); PINV(ea); }
                SB();
                if (i > 0) o[pu][0] = MFMA(vf[pb][0], asf(pw[pu]), o[pu][0]);
                if (lo) { ea[2] = __builtin_amdgcn_exp2f(ea[2]); ea[3] = __builtin_amdgcn_exp2f(ea[3]); PINV(ea); }
                if (hi) { eb[0] = __builtin_amdgcn_exp2f(eb[0]); eb[1] = __builtin_amdgcn_exp2f(eb[1]); PINV(eb); }
                SB();
                if (i > 0) o[pu][1] = MFMA(vf[pb][1], asf(pw[pu]), o[pu][1]);
                if (hi) { eb[2] = __builtin_amdgcn_exp2f(eb[2]); eb[3] = __builtin_amdgcn_exp2f(eb[3]); PINV(eb); }
                SB();
                if (i < 18) { pw[u] = (u32x4){lo ? pk2(ea[0], ea[1]) : 0u, lo ? pk2(ea[2], ea[3]) : 0u, hi ? pk2(eb[0], eb[1]) : 0u, hi ? pk2(eb[2], eb[3]) : 0u}; PINV(pw[u]); }
                SB();
            }
#pragma unroll
            for (int u = 0; u < 2; ++u) {
                const float il = __builtin_amdgcn_rcpf(o[u][1][3]);
                __builtin_amdgcn_raw_buffer_store_b128((u32x4){pk2(o[u][0][0] * il, o[u][0][1] * il), pk2(o[u][0][2] * il, o[u][0][3] * il), pk2(o[u][1][0] * il, o[u][1][1] * il), pk2(o[u][1][2] * il, 0.f)}, rs, ost + u * 16384, 0, 16);
            }
        }
        STAMP(7);
        const unsigned fo = WS_FO + (art * 16 + hd) * 128 + A.l0;
        WAITV(0);
        put_flag(rs, fo | last_of(L_CNT + 32, 4), ep);
        __builtin_amdgcn_s_setprio(0);
        if constexpr (SPARE) {
            if (A.last && wv == 0) {
                __builtin_amdgcn_s_sleep(64);
#pragma nounroll
                for (unsigned it = 0; it < SPIN_MAX; ++it) {
                    unsigned bad = 0;
                    asm volatile("" ::: "memory");
#pragma unroll
                    for (int i = 0; i < 4; ++i) { const u32x2 f = get_flag(rs, WS_FO + (i * 64 + lane2) * 128); bad |= (f[0] ^ ep[0]) | (f[1] ^ ep[1]); }
                    if (__builtin_amdgcn_ballot_w64(bad != 0) == 0) break;
                    __builtin_amdgcn_s_sleep(64);
                }
                put_flag(rs, WS_E + A.l0, ep);
            }
            asm volatile(".Lfk_end:");
            FIN();
            __builtin_amdgcn_endpgm();
        } else {
            xres = *(const GAS u32x4*)(A.x + oxo);
            const unsigned d0 = 4 * gb + q4b, d1 = 16 + 3 * gb + min(q4b, 2u);
            unsigned a0[4], a1[4], a2[4], a3[4];
#pragma unroll
            for (unsigned nt = 0; nt < 4; ++nt) {
                a0[nt] = L_WO + d0 * 128 + ((nt ^ ((d0 >> 1) & 3)) << 5) + p4b * 8; a1[nt] = L_WO + d1 * 128 + ((nt ^ ((d1 >> 1) & 3)) << 5) + p4b * 8;
                a2[nt] = a0[nt] + L_WO2; a3[nt] = a1[nt] + L_WO2;
                PINV(a0[nt]); PINV(a1[nt]); PINV(a2[nt]); PINV(a3[nt]);
            }
            const unsigned foff = WS_FO + ((rt64 >> 1) * 16 + r16b) * 128, obase = WS_O + (4 * rt64 + wv) * 16384 + lane2 * 16;
            HalfFrag wof[16][4];
#define RWO(i) do { constexpr int i_ = (i) < 9 ? (i) : (i) - 9, o_ = i_ * 3584; _Pragma("unroll") for (int nt = 0; nt < 4; ++nt) { \
                wof[i][nt].lo = ldstr(((i) < 9 ? a0 : a2)[nt ^ ((i_ & 1) << 1)] + o_); wof[i][nt].hi = ldstr(((i) < 9 ? a1 : a3)[nt ^ ((i_ & 1) << 1)] + o_); } } while (0)
            lds_wait(L_CNT + 8, 4);
            STAMP(30);
            RWO(0); RWO(1); RWO(2); RWO(3); RWO(4); RWO(5);
            SB();
            if (wv == 0) { __builtin_amdgcn_s_sleep(OSLP); spin_flags(rs, foff, ep); lds_arrive(L_CNT + 12); } else lds_wait(L_CNT + 12, 1);
            STAMP(9);
            u32x4 of[16];
#pragma unroll
            for (int i = 0; i < 16; ++i) of[i] = __builtin_amdgcn_raw_buffer_load_b128(rs, obase + (i & 3) * 1024, (i >> 2) * 4096, 16);
            SB();
            f32x4 ac[4] = {ZERO4, ZERO4, ZERO4, ZERO4};
#define MWO(i) do { _Pragma("unroll") for (int nt = 0; nt < 4; ++nt) ac[nt] = MFMA(join(wof[i][nt]), asf(of[i]), ac[nt]); } while (0)
            MWO(0); RWO(6); SB();
            MWO(1); RWO(7); SB();
            MWO(2); RWO(8); SB();
            MWO(3); RWO(9); SB();
            MWO(4); RWO(10); SB();
            MWO(5); RWO(11); SB();
            MWO(6); RWO(12); SB();
            MWO(7); RWO(13); SB();
            MWO(8); RWO(14); SB();
            MWO(9); RWO(15); SB();
            MWO(10); SB();
            MWO(11); SB();
            MWO(12); SB();
            MWO(13); SB();
            MWO(14); SB();
            MWO(15); SB();
#undef MWO
#undef RWO
            STAMP(8);
            BAR();
#pragma unroll
            for (int nt = 0; nt < 4; ++nt) *(LAS f32x4*)(size_t)(L_RING + (8 + wv) * UNIT + r16b * 256 + (((nt * 4 + gb) ^ r16b) << 4)) = ac[nt];
        }
    }
    if constexpr (!SPARE) {
        BAR();
        STAMP(10);
        const unsigned row = tid2 >> 3, rr = (row & 15) * 256 + ((((tid2 & 7) * 2) ^ (row & 15)) << 4);
        const unsigned ra = L_RING + (row >> 5) * 4 * UNIT + ((row >> 4) & 1) * 4096 + rr, r5 = L_RING + (8 + (row >> 4)) * UNIT + rr;
        f32x4 s0 = ldsf4(r5), s1 = ldsf4(r5 ^ 16);
#pragma unroll
        for (int f = 0; f < 4; ++f) { s0 += ldsf4(ra + f * UNIT); s1 += ldsf4((ra ^ 16) + f * UNIT); }
        const u32x4 bb = __builtin_bit_cast(u32x4, lds128(L_B2 + (tid2 & 7) * 16));
        u32x4 res;
#pragma unroll
        for (int j = 0; j < 4; ++j) {
            const unsigned xj = xres[j], bj = bb[j];
            const float u0 = j < 2 ? s0[2 * j] : s1[2 * j - 4], u1 = j < 2 ? s0[2 * j + 1] : s1[2 * j - 3];
            res[j] = pk2(u0 + bflo(xj) + bflo(bj), u1 + bfhi(xj) + bfhi(bj));
        }
        *(GAS u32x4*)(A.out + (rt64 * 64 + row) * 896 + A.cb * 128 + (tid2 & 7) * 16) = res;
    }
}

__global__ void __launch_bounds__(512, 2) fused_k(const char* __restrict__ x, const char* __restrict__ wqkv, const char* __restrict__ wo, const char* __restrict__ w1,
                                                  const char* __restrict__ b1, const char* __restrict__ w2, const char* __restrict__ b2, char* __restrict__ out, char* ws) {
    STAMP(0);
    const unsigned tid = threadIdx.x, lane = tid & 63, wv = __builtin_amdgcn_readfirstlane(tid >> 6);
    const unsigned bid = blockIdx.x, xcd = bid & 7, idx = bid >> 3;
    {
        u64 pc; unsigned len;
        asm volatile("s_getpc_b64 %0\n.Lfk_begin:\n\ts_mov_b32 %1, .Lfk_end-.Lfk_begin-256" : "=s"(pc), "=s"(len));
        gdma4(lane * 4, (const char*)(pc + min(idx * 1664 + wv * 256, len)), L_JUNK + wv * 256);
    }
    const unsigned rt = (xcd & 1) * 8 + (idx & 7), G = xcd >> 1, slot = G * 4 + (idx >> 3);
    const bool two = slot < 5;
    const unsigned qb0 = two ? 2 * slot : slot + 5;
    const unsigned blk1 = two ? 2 * slot + 1 : 2 * slot - 5;
    const unsigned fb2 = two ? slot : 2 * slot - 4;
    const unsigned drow = tid >> 3, dc = lane & 7;
    P1 P;
    P.wk = wv * 1024; P.c0 = (rt + slot) - 7 * (((rt + slot) * 37) >> 8); P.lane = lane;
    P.xv0 = (rt * 128 + drow) * 896 + ((dc ^ ((drow >> 1) & 7)) << 4); P.xv1 = P.xv0 + 64 * 896;
    const unsigned wsw1 = (dc ^ ((drow & 2) | ((drow >> 1) & 4))) << 4;
    P.v0 = drow * 2688 + wsw1 + qb0 * 128; P.v2 = drow * 3584 + wsw1;
    P.m1 = two ? 1344 : 1792; P.v1 = drow * 2 * P.m1 + wsw1 + blk1 * 128;
    const unsigned srow_off = (rt * 128 + slot * 8 + wv) * 896 + lane * 16 + (lane < 56 ? 0 : OOB);
    asm volatile("" : "+s"(x), "+s"(wqkv), "+s"(wo), "+s"(w1), "+s"(b1), "+s"(w2), "+s"(b2), "+s"(out), "+s"(ws));
    P.rx = mkrsrc(x, 2048 * 896); P.rq = mkrsrc(wqkv, 448 * 2688); P.rw = mkrsrc(w1, 448 * 3584); P.r1 = two ? P.rq : P.rw;
    const u64 e0 = *(const __attribute__((address_space(4))) u64*)(ws + WS_E);
    asm volatile("buffer_load_dwordx4 %0, %1, %2, 0 offen" : "=v"(P.srow) : "v"(srow_off), "s"(P.rx) : "memory");
    piece<0>(P); piece<1>(P); piece<2>(P); piece<3>(P);
    const bool spare = idx - (G == 0 ? 24u : 28u) < 4u;
    const bool extra = spare && G < 2;
    const unsigned p3 = spare ? 0 : (idx >= 28 ? idx - 4 : idx);
    const unsigned p37 = (p3 * 37) >> 8, rt64 = 4 * xcd + p37, cb = p3 - 7 * p37;
    {
        const unsigned w3 = wv & 3, sel = 27u | (fb2 << 8) | ((blk1 & 31) << 16) | (cb << 24);
        gdma4((lane & 31) * 4 + ((sel >> (8 * w3)) & 31) * 128, w3 == 3 ? b2 : b1, L_B1 + w3 * 256 + (wv >> 2) * 4096);
    }
    piece<4>(P); piece<5>(P); piece<6>(P); piece<7>(P);
    {
        const unsigned k = bid * 8 + wv, k3 = min(k, 2015u) - 1960;
        gdma4(k < 1960 ? k * 1024 + (lane & 7) * 128 : (k3 * 8 + (lane & 7)) * 3584, k < 1568 ? w2 : (k < 1960 ? wo - 1568 * 1024 : w1 + 27 * 128), L_JUNK + wv * 256);
    }
    P.v2 += fb2 * 128;
    STAMP(12);
    const unsigned r16 = lane & 15, g = lane >> 4, q4 = r16 >> 2, p4 = lane & 3;
    const unsigned slab = wv & 3, ch = wv >> 2, s1 = slab >> 1;
    const u64 ep64 = e0 * 0xD1342543DE82EF95ull + 0x9E3779B97F4A7C15ull;
    const u32x2 ep = {(unsigned)ep64, (unsigned)(ep64 >> 32)};
    const rsrc_t rs = __builtin_amdgcn_make_buffer_rsrc(ws, 0, (int)WS_END, 0x00020000);
    {
        const unsigned fl = r16 >> 1, key = ((q4 >> 1) & 1) | ((g & 1) << 1);
#pragma unroll
        for (unsigned e = 0; e < 2; ++e) {
            const unsigned ee = e ^ ch ^ (slab & 1);
            P.xa[e] = (slab >> 1) * UNIT + ((slab & 1) * 32 + r16) * 128 + (((4 * ee + g) ^ fl) << 4);
#pragma unroll
            for (unsigned nt = 0; nt < 2; ++nt) P.wa[e][nt] = ee * 4096 + (8 * g + q4) * 128 + (((2 * ch + (nt ^ s1)) ^ key) << 5) + p4 * 8;
        }
    }
    const unsigned tb = 4 * rt + slab;
    P.rs = rs; P.tag = ep[1]; P.gb = WS_GS + tb * 256; P.gs = WS_GS + (rt * 128 + slot * 8 + wv) * 8;
    unsigned qd[2][2];
#pragma unroll
    for (unsigned nt = 0; nt < 2; ++nt) {
        const unsigned nl = 32 * ch + 16 * (nt ^ s1);
        qd[0][nt] = qkv_dest(64 * qb0 + nl + r16, tb, g); qd[1][nt] = qkv_dest(64 * (two ? blk1 : qb0) + nl + r16, tb, g);
    }
    u32x4 ones4 = {0x3f803f80u, 0x3f803f80u, 0x3f803f80u, 0x3f803f80u}; PINV(ones4);
    const bf16x8 ones = asf(ones4);
    *(LAS u32x4*)(size_t)(L_CNT + (lane & 3) * 16) = (u32x4){0u, 0u, 0u, 0u};
    bf16x8 xf[14][2];
    f32x4 acc[2][2][2];
#pragma unroll
    for (int i = 0; i < 8; ++i) acc[i >> 2][(i >> 1) & 1][i & 1] = ZERO4;
    Epi E; E.csw = L_CS + (slab & 1) * 256 + (32 * ch + 16 * s1) * 4; E.s1 = s1;
    asm volatile("s_waitcnt vmcnt(6) lgkmcnt(0)\n\ts_barrier" : "+v"(P.srow)::"memory");
    if (two) pass_a<true>(xf, acc, ones, P, E DIAGARG);
    else pass_a<false>(xf, acc, ones, P, E DIAGARG);
    STAMP(1);
    __builtin_amdgcn_s_setprio(2);
    epi_qkv(acc[0], E.mq, E.rq, E.csq[0], qd[0], rs);
    if (two) epi_qkv(acc[1], E.mq, E.rq, E.csq[1], qd[1], rs);
    if (wv < 4) __builtin_amdgcn_s_setprio(0); else __builtin_amdgcn_s_setprio(1);
    STAMP(2);
    unsigned tid1 = threadIdx.x; PINV(tid1);
    const unsigned hst = WS_H + 2 * tb * 56 * 1024 + (tid1 & 63) * 16, l0 = (tid1 & 63) == 0 ? 0 : OOB;
    const unsigned fhw = WS_FH + rt * 28 * 128 + l0;
    PB B; B.rs = rs; B.ep = ep; B.two = two; B.rws = mkrsrc(ws, WS_END);
    B.f0 = WS_FQ + (rt * 21 + qb0) * 128 + l0; B.f1 = two ? B.f0 + 128 : fhw + blk1 * 128;
    if (!two) {
        const unsigned ta[2] = {64 * ch + 32 * s1 + ((tid1 & 48) >> 1), 64 * ch + 32 * (s1 ^ 1) + ((tid1 & 48) >> 1)};
        u32x4 od[2]; epi_ffn(acc[1], od, ta, L_CS + 512, 2, s1, E.mr, E.rstd);
#pragma unroll
        for (int tt = 0; tt < 2; ++tt) __builtin_amdgcn_raw_buffer_store_b128(od[tt], rs, hst + (2 * blk1 + ch) * 1024 + tt * 56 * 1024, 0, 16);
    }
    STAMP(15);
    Rest A; A.x = x; A.wo = wo; A.w2 = w2; A.out = out; A.ws = ws; A.wv = wv; A.hd = 2 * xcd + (idx & 1); A.art = idx >> 1; A.rt64 = rt64; A.cb = cb; A.fb2 = fb2; A.s1 = s1;
    A.hst = hst; A.fhw = fhw; A.l0 = l0; A.extra = extra; A.last = bid == 4 + 8 * 28;
    if (spare) { rest<true>(xf, ones4, P, B, E, A DIAGARG); __builtin_unreachable(); }
    rest<false>(xf, ones4, P, B, E, A DIAGARG);
    FIN();
}

extern "C" void kernel_launch(void* const* d_in, const int* in_sizes, int n_in, void* d_out, int out_size, void* d_ws, size_t ws_size, hipStream_t stream) {
    (void)in_sizes; (void)n_in; (void)out_size; (void)ws_size;
    (void)hipFuncSetAttribute((const void*)fused_k, hipFuncAttributeMaxDynamicSharedMemorySize, LDS_BYTES);
    fused_k<<<256, 512, LDS_BYTES, stream>>>((const char*)d_in[0], (const char*)d_in[1], (const char*)d_in[2], (const char*)d_in[4], (const char*)d_in[5],
                                             (const char*)d_in[6], (const char*)d_in[7], (char*)d_out, (char*)d_ws);
}
```

```cpp
#include <hip/hip_runtime.h>
#include <stdint.h>

typedef unsigned long long u64;
typedef short bf16x8 __attribute__((ext_vector_type(8)));
typedef short s16x4 __attribute__((ext_vector_type(4)));
typedef __bf16 b16x2 __attribute__((ext_vector_type(2)));
typedef float f32x4 __attribute__((ext_vector_type(4)));
typedef float f32x2 __attribute__((ext_vector_type(2)));
typedef unsigned u32x4 __attribute__((ext_vector_type(4)));
typedef unsigned u32x2 __attribute__((ext_vector_type(2)));
typedef __amdgpu_buffer_rsrc_t rsrc_t;
#define LAS __attribute__((address_space(3)))
#define GAS __attribute__((address_space(1)))
#define DI __device__ __forceinline__
#define RLX_AGENT __ATOMIC_RELAXED, __HIP_MEMORY_SCOPE_AGENT

constexpr unsigned WS_E = 0;
constexpr unsigned WS_FQ = 4096;
constexpr unsigned WS_FH = 49152;
constexpr unsigned WS_FO = 106496;
constexpr unsigned WS_GS = 139264;
constexpr unsigned WS_Q = 163840;
constexpr unsigned WS_K = WS_Q + 16 * 64 * 1792;
constexpr unsigned WS_V = WS_K + 16 * 64 * 1792;
constexpr unsigned WS_H = WS_V + 16 * 64 * 2048;
constexpr unsigned WS_O = WS_H + 128 * 56 * 1024;
constexpr unsigned WS_END = WS_O + 128 * 16 * 1024;

constexpr int UNIT = 8192;
constexpr int phys(int piece) { return (((piece % 19) + 3) % 19) * UNIT; }
constexpr int L_REG = 12 * UNIT;
constexpr int L_Q = 0, L_K = UNIT, L_V = 4 * UNIT, L_WO = 0, L_WO2 = 15 * UNIT;
constexpr int L_TB = 19 * UNIT;
constexpr int L_CS = L_TB, L_B1 = L_TB + 1024, L_B2 = L_B1 + 768, L_JUNK = L_TB + 2048, L_ST = L_TB + 6144;
constexpr int L_RING = 7 * UNIT, L_CNT = L_TB + 4096, L_CS2 = L_TB + 4608;
constexpr int rslot(int ks) { return ((ks & 3) >> 1) * 4 * UNIT + (ks & 1) * 4096; }
constexpr int LDS_BYTES = 163840;

#define FQ_AT 3
#define HF1_AT 6
#define ESLP 2
#define SLP 2
#define OSLP 0
constexpr unsigned SPIN_MAX = 400000;
constexpr unsigned OOB = 0x80000000u;

#define MFMA(a, b, c) __builtin_amdgcn_mfma_f32_16x16x32_bf16((a), (b), (c), 0, 0, 0)
#define WAITBAR(N) asm volatile("s_waitcnt vmcnt(%0) lgkmcnt(0)\n\ts_barrier" ::"i"(N) : "memory")
#define BAR() asm volatile("s_waitcnt lgkmcnt(0)\n\ts_barrier" ::: "memory")
#define WAITV(N) asm volatile("s_waitcnt vmcnt(%0)" ::"i"(N) : "memory")
#define SB() __builtin_amdgcn_sched_barrier(0)
#define PINV(x) asm volatile("" : "+v"(x))
#define PINS(x) asm volatile("" : "+s"(x))
#define ZERO4 ((f32x4){0.f, 0.f, 0.f, 0.f})

DI u32x4 mkrsrc(const char* p, unsigned bytes) { const u64 a = (u64)p; return (u32x4){(unsigned)a, (unsigned)(a >> 32), bytes, 0x20000u}; }
template <int DST> DI void dma16(unsigned voff, u32x4 r, unsigned soff, unsigned wk) {
    asm volatile("s_add_u32 m0, %3, %4\n\ts_nop 0\n\tbuffer_load_dwordx4 %0, %1, %2 offen lds" ::"v"(voff), "s"(r), "s"(soff), "s"(wk), "i"(DST) : "memory", "scc");
}
template <int DST, int SOFF> DI void dma16i(unsigned voff, u32x4 r, unsigned wk) {
    unsigned t;
    asm volatile("s_add_u32 m0, %3, %4\n\ts_mov_b32 %0, %5\n\tbuffer_load_dwordx4 %1, %2, %0 offen lds" : "=&s"(t) : "v"(voff), "s"(r), "s"(wk), "i"(DST), "i"(SOFF) : "memory", "scc");
}
template <int DST> DI void dma16c(unsigned voff, u32x4 r, unsigned wk) {
    asm volatile("s_add_u32 m0, %2, %3\n\ts_nop 0\n\tbuffer_load_dwordx4 %0, %1, 0 offen sc1 lds" ::"v"(voff), "s"(r), "s"(wk), "i"(DST) : "memory", "scc");
}
template <int DST> DI void dma16v(unsigned voff, u32x4 r, unsigned wk) {
    asm volatile("s_add_u32 m0, %2, %3\n\ts_mov_b32 exec_lo, 0x0fffffff\n\ts_mov_b32 exec_hi, 0x0fffffff\n\tbuffer_load_dwordx4 %0, %1, 0 offen sc1 lds\n\ts_mov_b64 exec, -1" ::"v"(voff), "s"(r), "s"(wk), "i"(DST) : "memory", "scc");
}
DI void gdma4(unsigned voff, const char* sbase, unsigned ldsdst) {
    asm volatile("s_mov_b32 m0, %2\n\ts_nop 0\n\tglobal_load_lds_dword %0, %1" ::"v"(voff), "s"(sbase), "s"(ldsdst) : "memory");
}
DI bf16x8 lds128(unsigned a) { return *(const LAS bf16x8*)(size_t)a; }
DI f32x4 ldsf4(unsigned a) { return *(const LAS f32x4*)(size_t)a; }
DI float ldsf(unsigned a) { return *(const LAS float*)(size_t)a; }
DI s16x4 ldstr(unsigned a) { return __builtin_amdgcn_ds_read_tr16_b64_v4i16((LAS s16x4*)(size_t)a); }
DI unsigned pk2(float a, float b) { f32x2 p = {a, b}; return __builtin_bit_cast(unsigned, __builtin_convertvector(p, b16x2)); }
DI float rbf(float a) { return __uint_as_float(pk2(a, 0.f) << 16); }
DI float bflo(unsigned u) { return __uint_as_float(u << 16); }
DI float bfhi(unsigned u) { return __uint_as_float(u & 0xffff0000u); }
DI bf16x8 asf(u32x4 v) { return __builtin_bit_cast(bf16x8, v); }
DI float redmax4(float v) {
    auto a = __builtin_amdgcn_permlane16_swap(__float_as_uint(v), __float_as_uint(v), false, false); v = __builtin_fmaxf(__uint_as_float(a[0]), __uint_as_float(a[1]));
    auto b = __builtin_amdgcn_permlane32_swap(__float_as_uint(v), __float_as_uint(v), false, false); return __builtin_fmaxf(__uint_as_float(b[0]), __uint_as_float(b[1])); }
template <int CTRL> DI float dppadd(float v) { return v + __uint_as_float(__builtin_amdgcn_update_dpp(0, __float_as_uint(v), CTRL, 0xf, 0xf, true)); }
DI float wavesum(float v) {
    v = dppadd<0xB1>(v); v = dppadd<0x4E>(v); v = dppadd<0x141>(v); v = dppadd<0x140>(v);
    auto a = __builtin_amdgcn_permlane16_swap(__float_as_uint(v), __float_as_uint(v), false, false); v = __uint_as_float(a[0]) + __uint_as_float(a[1]);
    auto b = __builtin_amdgcn_permlane32_swap(__float_as_uint(v), __float_as_uint(v), false, false); return __uint_as_float(b[0]) + __uint_as_float(b[1]); }
DI void put_flag(rsrc_t rs, unsigned off, u32x2 v) { __builtin_amdgcn_raw_buffer_store_b64(v, rs, off, 0, 16); }
DI u32x2 get_flag(rsrc_t rs, unsigned off) { return __builtin_amdgcn_raw_buffer_load_b64(rs, off, 0, 16); }
DI unsigned last_of(unsigned cnt, unsigned n) {
    unsigned r;
    asm volatile("s_mov_b64 exec, 1\n\tds_add_rtn_u32 %0, %1, %2\n\ts_mov_b64 exec, -1\n\ts_waitcnt lgkmcnt(0)" : "=&v"(r) : "v"(cnt), "v"(1u) : "memory");
    return (unsigned)__builtin_amdgcn_readfirstlane(r) == n - 1 ? 0u : 0x80000000u;
}
DI bool all_set(u32x2 f, u32x2 ep) { return __builtin_amdgcn_ballot_w64(((f[0] ^ ep[0]) | (f[1] ^ ep[1])) != 0) == 0; }
DI void spin_flags(rsrc_t rs, unsigned off, u32x2 ep) {
#pragma nounroll
    for (unsigned it = 0; it < SPIN_MAX; ++it) {
        asm volatile("" ::: "memory");
        if (all_set(get_flag(rs, off), ep)) break;
        __builtin_amdgcn_s_sleep(SLP);
    }
    asm volatile("" ::: "memory");
}
DI void lds_arrive(unsigned a) {
    asm volatile("s_waitcnt lgkmcnt(0)\n\ts_mov_b64 exec, 1\n\tds_add_u32 %0, %1\n\ts_mov_b64 exec, -1" ::"v"(a), "v"(1u) : "memory");
}
DI void lds_wait(unsigned a, unsigned n) {
#pragma nounroll
    for (unsigned it = 0; it < SPIN_MAX; ++it) {
        asm volatile("" ::: "memory");
        if (__builtin_amdgcn_readfirstlane(__hip_atomic_load((LAS unsigned*)(size_t)a, __ATOMIC_RELAXED, __HIP_MEMORY_SCOPE_WORKGROUP)) >= n) break;
        __builtin_amdgcn_s_sleep(2);
    }
    asm volatile("" ::: "memory");
}
DI unsigned vsel(bool c, unsigned a, unsigned b) { PINV(a); PINV(b); return c ? a : b; }
DI unsigned wrap7(unsigned v) { return v >= 7 ? v - 7 : v; }
constexpr int kstep(int f, int p) {
    if (p < 5) { const int i = 5 * f + p; return 10 + 4 * (i >> 1) + (i & 1); }
    const int i = 9 * f + p - 5;
    return i < 2 ? 50 + i : (i < 12 ? i - 2 : (i < 34 ? 12 + 4 * ((i - 12) >> 1) + (i & 1) : 20 + i));
}
constexpr u64 kpack(int f, int p0) { u64 v = 0; for (int p = p0; p < p0 + 8 && p < 14; ++p) v |= (u64)kstep(f, p) << (8 * (p - p0)); return v; }
DI u64 ksel(unsigned f, int p0) { return f == 0 ? kpack(0, p0) : (f == 1 ? kpack(1, p0) : (f == 2 ? kpack(2, p0) : kpack(3, p0))); }
#define HOLD 1
#define WO1 5
#define WOS 4
#define PRIO_A 3
constexpr int fw(int s) { return s + 3 < 14 ? s + 3 : -1; }
constexpr int f_nw(int s) { return s == 0 ? 4 : (s == 1 ? 3 : ((s == WO1 || s == WO1 + 1) ? 4 : 0)); }
constexpr int f_np(int s) { return (s == 0 ? 1 : 0) + (s == 7 ? 1 : 0) + (s == 10 ? 1 : 0); }
constexpr int f_all(int s) { return 8 * (fw(s) >= 0) + f_nw(s) + f_np(s); }
constexpr int f_cum(int s) { int c = 17; for (int i = 0; i < s; ++i) c += f_all(i); return c; }
constexpr int f_wend(int k) { return k == 2 ? 16 : f_cum(k - 3) + 4; }
constexpr int f_hend(int k) { return k < 3 ? 4 + 4 * k : f_cum(k - 3) + 8; }
constexpr int f_wait(int s) { const int a = f_cum(s) - f_hend(s), b = (s >= 1 && s <= 12) ? f_cum(s) - f_wend(s + 1) : 63; return a < b ? a : b; }

#define STAMP(i) do { } while (0)
#define FIN() do { } while (0)
#define DIAGPAR
#define DIAGARG

struct HalfFrag { s16x4 lo, hi; };
DI bf16x8 join(const HalfFrag& h) { return __builtin_shufflevector(h.lo, h.hi, 0, 1, 2, 3, 4, 5, 6, 7); }
DI void rd_tr(HalfFrag& h, unsigned base, unsigned c, unsigned d) {
    if (c + d < 65536) { h.lo = ldstr(base + c); h.hi = ldstr(base + c + d); }
    else { unsigned a = base + c; PINV(a); h.lo = ldstr(a); h.hi = ldstr(a + d); }
}

struct P1 {
    unsigned xa[2], wa[2][2];
    u32x4 rx, rq, r1, rw;
    unsigned xv0, xv1, v0, v1, v2;
    unsigned m1, wk, c0;
    rsrc_t rs; unsigned gb, gs, tag, lane;
    u32x4 srow; float sv[8], sm, sq;
};
template <int N> DI void piece(const P1& P) {
    if constexpr (N < 28) {
        const unsigned kc = wrap7(P.c0 + (N >> 2)) * 128;
        if constexpr ((N & 3) == 0) dma16<phys(N)>(P.xv0, P.rx, kc, P.wk);
        else if constexpr ((N & 3) == 1) dma16<phys(N)>(P.xv1, P.rx, kc, P.wk);
        else if constexpr ((N & 3) == 2) dma16<phys(N)>(P.v0, P.rq, kc * 1344, P.wk);
        else dma16<phys(N)>(P.v1, P.r1, kc * P.m1, P.wk);
    } else if constexpr (N < 35) {
        dma16<phys(N)>(P.v2, P.rw, wrap7(P.c0 + (N - 28)) * (128 * 1792), P.wk);
    }
}
template <int S> DI void rd_ax(bf16x8 (&xf)[14][2], int tt, const P1& P) { if constexpr (S < 14) xf[S][tt] = lds128(P.xa[S & 1] + phys(4 * (S >> 1)) + tt * 2048); }
template <int S> DI void rd_aw(HalfFrag (&wf)[2][2][2], int b, int nt, const P1& P) { if constexpr (S < 14) rd_tr(wf[S & 1][b][nt], P.wa[S & 1][nt], phys(4 * (S >> 1) + 2 + b), 512); }
#define A_PRE 8
#define A_SCHED {4, 4, 4, 2, 2, 2, 2, 2, 2, 2, 1, 0, 0, 0}
constexpr int A_CNT[14] = A_SCHED;
constexpr int A_GS = 2, A_GL = 10;
constexpr int a_first(int s) { int n = A_PRE; for (int i = 0; i < s; ++i) n += A_CNT[i]; return n < 35 ? n : 35; }
constexpr int a_count(int s) { return a_first(s + 1) - a_first(s); }
constexpr int a_wait(int c) {
    int n = a_first(2 * c - 1) - (4 * c + 4);
    if (4 * c + 3 < A_PRE) n += 1;
    if (A_GS < 2 * c - 1 && 4 * c + 3 < a_first(A_GS + 1)) n += 1;
    if (A_GL < 2 * c - 1) n += 1;
    return n;
}
constexpr bool a_ok() { for (int s = 0; s < 14; ++s) { const int last = a_first(s + 1) - 1; if (a_count(s) > 4 || (last >= 19 && a_count(s) > 0 && (last - 19) / 4 > (s + 1) / 2 - 1)) return false; if ((s & 1) && a_first(s) < 2 * (s + 1) + 4 && s < 13) return false; } return a_first(13) == 35; }
static_assert(a_ok(), "pass A schedule");
DI u32x2 ld_gran(const P1& P) { unsigned ln = threadIdx.x; PINV(ln); return __builtin_amdgcn_raw_buffer_load_b64(P.rs, P.gb + (ln & 31) * 8, 0, 16); }
template <int S> DI void stats_stage(P1& P) {
    if constexpr (S == 0) {
#pragma unroll
        for (int j = 0; j < 4; ++j) { const unsigned u = P.srow[j]; P.sv[2 * j] = bflo(u); P.sv[2 * j + 1] = bfhi(u); }
        P.sm = wavesum(((P.sv[0] + P.sv[1]) + (P.sv[2] + P.sv[3])) + ((P.sv[4] + P.sv[5]) + (P.sv[6] + P.sv[7]))) * (1.f / 448.f); PINV(P.sm);
    } else if constexpr (S == 1) {
        float q0 = 0.f, q1 = 0.f;
#pragma unroll
        for (int j = 0; j < 4; ++j) { const float d0 = P.sv[j] - P.sm, d1 = P.sv[j + 4] - P.sm; q0 = __builtin_fmaf(d0, d0, q0); q1 = __builtin_fmaf(d1, d1, q1); }
        P.sq = P.lane < 56 ? q0 + q1 : 0.f; PINV(P.sq);
    } else if constexpr (S == 2) { P.sq = wavesum(P.sq) * (1.f / 448.f); PINV(P.sq);
    } else {
        const unsigned sdm = pk2(__builtin_amdgcn_sqrtf(rbf(rbf(P.sq) + 1e-5f)), P.sm);
        put_flag(P.rs, P.gs + (P.lane == 0 ? 0 : 0x80000000u), (u32x2){sdm, P.tag});
    }
}
struct Epi { float mr[2], rstd[2], csq[2][2]; f32x4 mq[2], rq[2]; unsigned csw; int s1; };
DI void gran_check(u32x2& R, const P1& P) {
#pragma nounroll
    for (unsigned it = 0; it < SPIN_MAX; ++it) {
        if (__builtin_amdgcn_ballot_w64(R[1] != P.tag) == 0) break;
        __builtin_amdgcn_s_sleep(1);
        asm volatile("" ::: "memory");
        R = ld_gran(P);
    }
}
template <bool B1Q, int S>
DI void step_a(bf16x8 (&xf)[14][2], HalfFrag (&wf)[2][2][2], f32x4 (&acc)[2][2][2], f32x4 (&cs)[2], bf16x8 ones, P1& P, u32x2& R, Epi& E DIAGPAR) {
    if constexpr (S & 1) {
        constexpr int c = (S + 1) >> 1;
        if constexpr (c < 7) WAITBAR(a_wait(c)); else WAITBAR(0);
        STAMP(16 + c);
    }
    constexpr int f = a_first(S), n = a_count(S);
    float ca[2][2], cb_[2][2];
    if constexpr (S == 12) { cs[0] = MFMA(ones, join(wf[0][0][0]), cs[0]); cs[1] = MFMA(ones, join(wf[0][1][0]), cs[1]); SB(); }
    if constexpr (S == 13) {
        const unsigned st = L_ST + (P.wk >> 2);
        unsigned ln = threadIdx.x; PINV(ln);
#pragma unroll
        for (int tt = 0; tt < 2; ++tt) {
            E.rq[tt] = ldsf4(st + tt * 64 + (ln & 48)); E.mq[tt] = ldsf4(st + 128 + tt * 64 + (ln & 48));
            E.rstd[tt] = ldsf(st + tt * 64 + (ln & 15) * 4); E.mr[tt] = ldsf(st + 128 + tt * 64 + (ln & 15) * 4);
        }
#pragma unroll
        for (int i = 0; i < 4; ++i) { const unsigned a = (E.csw & ~(256u | 64u)) + (i >> 1) * 512 + ((i & 1) ^ E.s1) * 64; ca[i >> 1][i & 1] = ldsf(a); cb_[i >> 1][i & 1] = ldsf(a + 256); }
        SB();
    }
#define MA(b, nt, tt) do { acc[b][nt][tt] = (b == 0 || B1Q) ? MFMA(xf[S][tt], join(wf[S & 1][b][nt]), acc[b][nt][tt]) : MFMA(join(wf[S & 1][b][nt]), xf[S][tt], acc[b][nt][tt]); } while (0)
    MA(0, 0, 0); rd_ax<S + 1>(xf, 0, P); SB();
    MA(0, 0, 1); rd_aw<S + 1>(wf, 0, 0, P); SB();
    MA(0, 1, 0); if constexpr (n > 0) piece<f>(P); SB();
    MA(0, 1, 1); rd_ax<S + 1>(xf, 1, P); SB();
    if constexpr (n > 2) { piece<f + 1>(P); SB(); }
    MA(1, 0, 0); rd_aw<S + 1>(wf, 0, 1, P); SB();
    MA(1, 0, 1); if constexpr (n > 1) piece<f + (n > 2 ? 2 : 1)>(P); SB();
    if constexpr (S == 12) {
        unsigned t1 = threadIdx.x; PINV(t1);
        E.csw += (t1 & 15) * 4;
        *(LAS float*)(size_t)(E.csw) = cs[0][0]; *(LAS float*)(size_t)(E.csw + 512) = cs[1][0]; SB();
        gran_check(R, P);
        const float rs_ = __builtin_amdgcn_rcpf(bflo(R[0])); const unsigned st = L_ST + (P.wk >> 2) + (t1 & 31) * 4;
        *(LAS float*)(size_t)(st) = rs_; *(LAS float*)(size_t)(st + 128) = bfhi(R[0]) * rs_; SB();
    }
    if constexpr (S == 13) {
#pragma unroll
        for (int i = 0; i < 4; ++i) { E.csq[i >> 1][i & 1] = ca[i >> 1][i & 1] + cb_[i >> 1][i & 1]; PINV(E.csq[i >> 1][i & 1]); }
        SB();
    }
    MA(1, 1, 0); rd_aw<S + 1>(wf, 1, 0, P); SB();
    MA(1, 1, 1); rd_aw<S + 1>(wf, 1, 1, P); SB();
#undef MA
    if constexpr ((S & 1) == 0 && S < 12) { cs[0] = MFMA(ones, join(wf[0][0][0]), cs[0]); cs[1] = MFMA(ones, join(wf[0][1][0]), cs[1]); SB(); }
    if constexpr (n > 3) { piece<f + 3>(P); SB(); }
    if constexpr (S <= A_GS) { stats_stage<S + 1>(P); SB(); }
    if constexpr (S == A_GL) { R = ld_gran(P); SB(); }
    if constexpr (S < 13) step_a<B1Q, S + 1>(xf, wf, acc, cs, ones, P, R, E DIAGARG);
}
template <bool B1Q>
DI void pass_a(bf16x8 (&xf)[14][2], f32x4 (&acc)[2][2][2], bf16x8 ones, P1& P, Epi& E DIAGPAR) {
    HalfFrag wf[2][2][2]; u32x2 R; f32x4 cs[2] = {ZERO4, ZERO4};
    STAMP(13);
    rd_ax<0>(xf, 0, P); rd_aw<0>(wf, 0, 0, P); rd_ax<0>(xf, 1, P); rd_aw<0>(wf, 0, 1, P); rd_aw<0>(wf, 1, 0, P); rd_aw<0>(wf, 1, 1, P); SB();
    step_a<B1Q, 0>(xf, wf, acc, cs, ones, P, R, E DIAGARG);
}
DI unsigned qkv_dest(unsigned n, unsigned tb, unsigned g) {
    const unsigned hs = (n * 2341u) >> 16, d = n - hs * 28u;
    const unsigned isv = hs >> 5, pl = 448 + 64 * isv;
    unsigned o = WS_Q - isv * (32 * 64 * 256) + (hs * 256 + tb * 4 + g) * pl + d * 16 + ((d >> 3) & 1 & ~isv);
    PINV(o); return o;
}
DI void epi_qkv(const f32x4 (&acc)[2][2], const f32x4 (&mr)[2], const f32x4 (&rq)[2], const float (&cs)[2], const unsigned (&q)[2], rsrc_t rs) {
#pragma unroll
    for (int nt = 0; nt < 2; ++nt) {
        unsigned w[2][2];
#pragma unroll
        for (int tt = 0; tt < 2; ++tt) {
            const f32x4 v = acc[nt][tt] * rq[tt] - mr[tt] * cs[nt];
            w[tt][0] = pk2(v[0], v[1]); w[tt][1] = pk2(v[2], v[3]);
        }
        const bool sw = q[nt] & 1;
        const u32x4 o = {sw ? w[1][0] : w[0][0], sw ? w[1][1] : w[0][1], sw ? w[0][0] : w[1][0], sw ? w[0][1] : w[1][1]};
        __builtin_amdgcn_raw_buffer_store_b128(o, rs, q[nt] & ~15u, 0, 16);
    }
}
DI void epi_ffn(const f32x4 (&acc)[2][2], u32x4 (&o)[2], const unsigned (&ta)[2], int csb, int b1i, bool s1, const float (&mr)[2], const float (&rstd)[2]) {
    f32x4 cs[2], bb[2];
#pragma unroll
    for (int nt = 0; nt < 2; ++nt) {
        cs[nt] = ldsf4(csb + ta[nt] * 2) + ldsf4(csb + 256 + ta[nt] * 2);
        const u32x2 b = *(const LAS u32x2*)(size_t)(L_B1 + b1i * 256 + ta[nt]);
        bb[nt] = (f32x4){bflo(b[0]), bfhi(b[0]), bflo(b[1]), bfhi(b[1])};
    }
#pragma unroll
    for (int tt = 0; tt < 2; ++tt) {
        unsigned w[2][2];
#pragma unroll
        for (int nt = 0; nt < 2; ++nt) {
            f32x4 u = acc[nt][tt] * rstd[tt] + (bb[nt] - mr[tt] * cs[nt]);
            u = __builtin_elementwise_max(u, ZERO4); u = u * u;
            w[nt][0] = pk2(u[0], u[1]); w[nt][1] = pk2(u[2], u[3]);
        }
        o[tt] = s1 ? (u32x4){w[1][0], w[1][1], w[0][0], w[0][1]} : (u32x4){w[0][0], w[0][1], w[1][0], w[1][1]};
    }
}
struct PB { u32x4 rws; rsrc_t rs; u32x2 ep; unsigned f0, f1, qpo; u32x2 pl[2]; bool two; };
DI void poll_asm(u32x2& d, unsigned off, u32x4 r) { asm volatile("buffer_load_dwordx2 %0, %1, %2, 0 offen sc1" : "=v"(d) : "v"(off), "s"(r) : "memory"); }
DI void flag_both(const PB& B) { const unsigned lo = last_of(L_CNT + 16, 8); put_flag(B.rs, B.f0 | lo, B.ep); put_flag(B.rs, B.f1 | lo, B.ep); }
template <bool X27, int S>
DI void step_x(const bf16x8 (&xf)[14][2], HalfFrag (&wf)[3][4], f32x4 (&ac)[4][2], f32x4 (&cs)[4], bf16x8 ones, const unsigned (&wa)[2][4], PB& B DIAGPAR) {
    if constexpr (X27 && S == 7) { WAITV(0); put_flag(B.rs, B.f1 | last_of(L_CNT + 24, 4), B.ep); lds_arrive(L_CNT + 40); lds_wait(L_CNT + 40, 4); }
#pragma unroll
    for (int nt = 0; nt < 4; ++nt) {
        ac[nt][0] = MFMA(join(wf[S % 3][nt]), xf[S][0], S == 0 ? ZERO4 : ac[nt][0]);
        if constexpr (S < 12) rd_tr(wf[(S + 2) % 3][nt], wa[S & 1][nt], ((S + 2) >> 1) * UNIT, 512);
        SB();
        ac[nt][1] = MFMA(join(wf[S % 3][nt]), xf[S][1], S == 0 ? ZERO4 : ac[nt][1]);
        if constexpr (X27) cs[nt] = MFMA(join(wf[S % 3][nt]), ones, S == 0 ? ZERO4 : cs[nt]);
        SB();
    }
    if constexpr (!X27) {
        if constexpr (S == FQ_AT) { WAITV(0); flag_both(B); STAMP(25); SB(); }
    }
    if constexpr (S < 13) step_x<X27, S + 1>(xf, wf, ac, cs, ones, wa, B DIAGARG);
}
DI void pre_x(HalfFrag (&wf)[3][4], unsigned (&wa)[2][4], const P1& P, unsigned c32, unsigned base) {
#pragma unroll
    for (int i = 0; i < 8; ++i) { wa[i >> 2][i & 3] = (P.wa[i >> 2][0] ^ ((c32 ^ (i & 3)) << 5)) + base; PINV(wa[i >> 2][i & 3]); }
#pragma unroll
    for (int i = 0; i < 8; ++i) rd_tr(wf[i >> 2][i & 3], wa[i >> 2][i & 3], 0, 512);
    SB();
}
DI void epi_x(const f32x4 (&ac)[4][2], const f32x4 (&cs)[4], unsigned b1a, const Epi& E, rsrc_t rs, unsigned dst, int c0, int c1) {
#pragma unroll
    for (int c = c0; c < c1; ++c) {
        f32x4 bb[2];
#pragma unroll
        for (int n = 0; n < 2; ++n) { const u32x2 b = *(const LAS u32x2*)(size_t)(b1a + c * 64 + n * 32); bb[n] = (f32x4){bflo(b[0]), bfhi(b[0]), bflo(b[1]), bfhi(b[1])}; }
#pragma unroll
        for (int tt = 0; tt < 2; ++tt) {
            f32x4 u0 = ac[2 * c][tt] * E.rstd[tt] + (bb[0] - E.mr[tt] * cs[2 * c]), u1 = ac[2 * c + 1][tt] * E.rstd[tt] + (bb[1] - E.mr[tt] * cs[2 * c + 1]);
            u0 = __builtin_elementwise_max(u0, ZERO4); u0 = u0 * u0; u1 = __builtin_elementwise_max(u1, ZERO4); u1 = u1 * u1;
            __builtin_amdgcn_raw_buffer_store_b128((u32x4){pk2(u0[0], u0[1]), pk2(u0[2], u0[3]), pk2(u1[0], u1[1]), pk2(u1[2], u1[3])}, rs, dst + c * 1024 + tt * 56 * 1024, 0, 16);
        }
    }
}

struct PF { u32x4 rw2, rwo, rws; rsrc_t rs; u32x2 ep, fl, fl2; u64 t0, t1; unsigned v[4], hv[4], vo, ring, fbo, flo, flo2; const char* xp; u32x4 xres; unsigned wb[4]; };
DI unsigned f_ks(const PF& F, int p) { return (unsigned)(p < 8 ? F.t0 >> (8 * p) : F.t1 >> (8 * (p - 8))) & 63u; }
template <int KS> DI void f_ldh(u32x4 (&hf)[14][4], int tt, const PF& F) {
    if constexpr (KS >= 0) { asm volatile("buffer_load_dwordx4 %0, %1, %2, %3 offen sc1" : "=v"(hf[KS][tt]) : "v"(F.hv[tt]), "s"(F.rws), "s"(f_ks(F, KS) * 1024) : "memory"); SB(); }
}
template <int KS> DI void f_rdw(HalfFrag (&wf)[2][4], int nt, const PF& F) { if constexpr (KS < 14) rd_tr(wf[KS & 1][nt], F.wb[nt], rslot(KS), 2048); }
template <int KS, int I> DI void f_dma(const PF& F) { if constexpr (KS >= 0) { dma16<L_RING + rslot(KS) + I * 1024>(F.v[I], F.rw2, f_ks(F, KS) * (32 * 896), F.ring); SB(); } }
template <int S, int I> DI void f_wod(const PF& F) {
    if constexpr (I < f_nw(S)) {
        if constexpr (S == WO1 && I == 0) lds_wait(L_CNT + 4, 4);
        if constexpr (S < 2) dma16i<L_WO2 + (4 * S + I) * 4096, (252 + (4 * S + I) * 32) * 896>(F.vo, F.rwo, F.ring >> 3);
        else dma16i<L_WO + (4 * (S - WO1) + I) * 4096, (4 * (S - WO1) + I) * 32 * 896>(F.vo, F.rwo, F.ring >> 3);
        SB();
    }
}
template <int S>
DI void step_f(u32x4 (&hf)[14][4], HalfFrag (&wf)[2][4], f32x4 (&ac)[4][4], PF& F DIAGPAR) {
    STAMP(32 + S);
    asm volatile("s_waitcnt vmcnt(%4)" : "+v"(hf[S][0]), "+v"(hf[S][1]), "+v"(hf[S][2]), "+v"(hf[S][3]) : "i"(f_wait(S)) : "memory");
    if constexpr (S == WO1 + WOS) { WAITV(f_cum(S) - (f_cum(WO1 + 2) - f_np(WO1 + 1))); lds_arrive(L_CNT + 8); }
#define MF(tt, nt) do { ac[tt][nt] = MFMA(join(wf[S & 1][nt]), asf(hf[S][tt]), S == 0 ? ZERO4 : ac[tt][nt]); } while (0)
    MF(0, 0); f_dma<fw(S), 0>(F); MF(0, 1); SB(); f_dma<fw(S), 1>(F); MF(0, 2); SB(); f_dma<fw(S), 2>(F); MF(0, 3); SB(); f_dma<fw(S), 3>(F);
    MF(1, 0); f_rdw<S + 1>(wf, 0, F); SB(); MF(1, 1); f_rdw<S + 1>(wf, 1, F); SB();
    MF(1, 2); f_rdw<S + 1>(wf, 2, F); SB(); MF(1, 3); f_rdw<S + 1>(wf, 3, F); SB();
    if constexpr (S == 2) { asm volatile("s_waitcnt vmcnt(%1)" : "+v"(F.fl) : "i"(f_all(1) + 4) : "memory"); if (!all_set(F.fl, F.ep)) spin_flags(F.rs, F.flo, F.ep); }
    if constexpr (S == 9) { asm volatile("s_waitcnt vmcnt(%1)" : "+v"(F.fl2) : "i"(f_all(8) + 4) : "memory"); if (!all_set(F.fl2, F.ep)) spin_flags(F.rs, F.flo2, F.ep); }
    f_ldh<fw(S)>(hf, 0, F); MF(2, 0); SB(); f_ldh<fw(S)>(hf, 1, F); MF(2, 1); SB(); f_ldh<fw(S)>(hf, 2, F); MF(2, 2); SB(); f_ldh<fw(S)>(hf, 3, F); MF(2, 3); SB();
    f_wod<S, 0>(F); MF(3, 0); SB(); f_wod<S, 1>(F); MF(3, 1); SB(); f_wod<S, 2>(F); MF(3, 2); SB(); f_wod<S, 3>(F); MF(3, 3); SB();
#undef MF
    if constexpr (S == 0) { poll_asm(F.fl, F.flo, F.rws); SB(); }
    if constexpr (S == 7) { poll_asm(F.fl2, F.flo2, F.rws); SB(); }
    if constexpr (S == 10) { F.xres = *(const GAS u32x4*)F.xp; SB(); }
    if constexpr (S < 13) step_f<S + 1>(hf, wf, ac, F DIAGARG);
}

struct Rest { const char *x, *wo, *w2; char *out, *ws; unsigned wv, hd, art, rt64, cb, fb2, s1, hst, fhw, l0; bool extra, last; };
DI void final_sum(const Rest& A, u32x4 xres DIAGPAR) {
    {
        unsigned tid2 = threadIdx.x; PINV(tid2); const unsigned rt64 = A.rt64;
        BAR();
        STAMP(10);
        const unsigned row = tid2 >> 3, rr = (row & 15) * 256 + ((((tid2 & 7) * 2) ^ (row & 15)) << 4);
        const unsigned ra = L_RING + (row >> 5) * 4 * UNIT + ((row >> 4) & 1) * 4096 + rr, r5 = L_RING + (8 + (row >> 4)) * UNIT + rr;
        f32x4 s0 = ldsf4(r5), s1 = ldsf4(r5 ^ 16);
#pragma unroll
        for (int f = 0; f < 4; ++f) { s0 += ldsf4(ra + f * UNIT); s1 += ldsf4((ra ^ 16) + f * UNIT); }
        const u32x4 bb = __builtin_bit_cast(u32x4, lds128(L_B2 + (tid2 & 7) * 16));
        u32x4 res;
#pragma unroll
        for (int j = 0; j < 4; ++j) {
            const unsigned xj = xres[j], bj = bb[j];
            const float u0 = j < 2 ? s0[2 * j] : s1[2 * j - 4], u1 = j < 2 ? s0[2 * j + 1] : s1[2 * j - 3];
            res[j] = pk2(u0 + bflo(xj) + bflo(bj), u1 + bfhi(xj) + bfhi(bj));
        }
        *(GAS u32x4*)(A.out + (rt64 * 64 + row) * 896 + A.cb * 128 + (tid2 & 7) * 16) = res;
    }
    FIN();
    __builtin_amdgcn_endpgm();
}
template <bool SPARE>
DI void rest(const bf16x8 (&xf)[14][2], u32x4 ones4, const P1& P, PB& B, const Epi& E, const Rest& A DIAGPAR) {
    const bf16x8 ones = asf(ones4); const rsrc_t rs = B.rs; const u32x2 ep = B.ep; const unsigned wv = A.wv, hd = A.hd, art = A.art, rt64 = A.rt64, w4 = wv & 3;
    unsigned tid2 = threadIdx.x; PINV(tid2);
    const unsigned lane2 = tid2 & 63, r16b = lane2 & 15, gb = lane2 >> 4, q4b = r16b >> 2, p4b = lane2 & 3;
    u32x4 xres;
    if (wv >= 4) {
        const unsigned fbo = A.fhw + A.fb2 * 128;
        {
            unsigned wa[2][4]; HalfFrag wf[3][4]; f32x4 ac[4][2], cs[4];
            pre_x(wf, wa, P, 2 + A.s1, L_REG);
            STAMP(24);
            step_x<false, 0>(xf, wf, ac, cs, ones, wa, B DIAGARG);
            lds_arrive(L_CNT + 52);
            STAMP(3);
            lds_wait(L_CNT + 48, 4);
#pragma unroll
            for (int nt = 0; nt < 4; ++nt) cs[nt] = ldsf4(L_CS2 + nt * 64 + gb * 16);
            epi_x(ac, cs, L_B1 + 256 + gb * 8, E, rs, A.hst + A.fb2 * 2048, 0, 2);
            __builtin_amdgcn_s_setprio(0);
            STAMP(29);
        }
        if constexpr (!SPARE) {
            const unsigned oxo = (rt64 * 64 + (tid2 >> 3)) * 896 + A.cb * 128 + (tid2 & 7) * 16;
            PF F; F.rw2 = mkrsrc(A.w2, 1792 * 896); F.rwo = mkrsrc(A.wo, 448 * 896); F.rws = B.rws; F.rs = rs; F.ep = ep; F.ring = w4 * UNIT; F.xp = A.x + oxo; F.fbo = fbo;
            F.t0 = ksel(w4, 0); F.t1 = ksel(w4, 8);
#pragma unroll
            for (int i = 0; i < 4; ++i) {
                F.hv[i] = WS_H + (4 * rt64 + i) * (56 * 1024) + lane2 * 16;
                F.v[i] = ((lane2 >> 3) + 8 * i) * 896 + (((lane2 ^ (lane2 >> 3 & 6)) & 7) << 4) + A.cb * 128;
            }
            lds_wait(L_CNT + 60, 4);
            u32x4 hf[14][4];
#pragma unroll
            for (int i = 0; i < 4; ++i) f_ldh<0>(hf, i, F);
#pragma unroll
            for (int i = 0; i < 4; ++i) f_ldh<1>(hf, i, F);
#pragma unroll
            for (int i = 0; i < 4; ++i) f_ldh<2>(hf, i, F);
            lds_wait(L_CNT + 52, 4);
            f_dma<2, 0>(F); f_dma<2, 1>(F); f_dma<2, 2>(F); f_dma<2, 3>(F);
            WAITV(16); put_flag(rs, fbo | last_of(L_CNT + 24, 4), ep);
            F.vo = F.v[0] + w4 * 7168;
            const unsigned fhb = WS_FH + (rt64 >> 1) * (28 * 128), p7 = 5 + min(lane2, 6u);
            F.flo = fhb + (((unsigned)(p7 < 8 ? F.t0 >> (8 * p7) : F.t1 >> (8 * (p7 - 8))) & 63u) >> 1) * 128;
            F.flo2 = fhb + (((unsigned)(F.t1 >> (32 + 8 * (lane2 & 1))) & 63u) >> 1) * 128;
            const unsigned key3 = (2 * gb + (q4b >> 1)) & 3;
#pragma unroll
            for (unsigned nt = 0; nt < 4; ++nt) { F.wb[nt] = L_RING + F.ring + (4 * gb + q4b) * 128 + ((nt ^ key3) << 5) + p4b * 8; PINV(F.wb[nt]); }
            HalfFrag wf[2][4]; f32x4 ac[4][4];
#pragma unroll
            for (int nt = 0; nt < 4; ++nt) f_rdw<0>(wf, nt, F);
            STAMP(5);
            step_f<0>(hf, wf, ac, F DIAGARG);
            STAMP(8);
#pragma unroll
            for (int i = 0; i < 16; ++i) *(LAS f32x4*)(size_t)(L_RING + F.ring + (i >> 3) * 4 * UNIT + ((i >> 2) & 1) * 4096 + r16b * 256 + ((((i & 3) * 4 + gb) ^ r16b) << 4)) = ac[i >> 2][i & 3];
            BAR();
            final_sum(A, F.xres DIAGARG);
        } else {
            if (!A.extra) { WAITV(0); put_flag(rs, fbo | last_of(L_CNT + 24, 4), ep); }
            else {
                const unsigned v1 = P.v2 + (27 - A.fb2) * 128, v0 = v1 - 32 * 3584;
                lds_wait(L_CNT + 52, 4); lds_wait(L_CNT + 60, 4);
#define X27(i) do { const unsigned so = wrap7(P.c0 + i) * (128 * 1792); dma16<L_RING + i * UNIT - 4096>(v0, P.rw, so, P.wk); dma16<L_RING + i * UNIT>(v1, P.rw, so, P.wk); } while (0)
                X27(5); X27(6);
#undef X27
                B.f1 = fbo;
                unsigned wa[2][4]; HalfFrag wf[3][4]; f32x4 ac[4][2], cs[4];
                pre_x(wf, wa, P, 2 + A.s1, L_RING);
                step_x<true, 0>(xf, wf, ac, cs, ones, wa, B DIAGARG);
                epi_x(ac, cs, L_B1 + gb * 8, E, rs, A.hst + 27 * 2048, 0, 2);
                WAITV(0);
                put_flag(rs, (A.fhw + 27 * 128) | last_of(L_CNT + 28, 4), ep);
            }
            FIN();
            __builtin_amdgcn_endpgm();
        }
    } else {
        const unsigned oxo = (rt64 * 64 + (tid2 >> 3)) * 896 + A.cb * 128 + (tid2 & 7) * 16;
        HalfFrag cf[14];
        {
            unsigned a[2];
#pragma unroll
            for (int e = 0; e < 2; ++e) { a[e] = (P.wa[e][0] ^ ((A.s1 ^ wv) << 5)) + L_REG; PINV(a[e]); }
#pragma unroll
            for (int i = 0; i < 14; ++i) rd_tr(cf[i], a[i & 1], (i >> 1) * UNIT, 512);
        }
        __builtin_amdgcn_s_setprio(2);
        {
            f32x4 c2 = ZERO4, c3 = ZERO4;
#pragma unroll
            for (int i = 0; i < 7; ++i) { c2 = MFMA(ones, join(cf[2 * i]), c2); c3 = MFMA(ones, join(cf[2 * i + 1]), c3); }
            *(LAS float*)(size_t)(L_CS2 + (wv * 16 + r16b) * 4) = c2[0] + c3[0];
            lds_arrive(L_CNT + 48);
        }
        STAMP(24);
        WAITV(0);
        flag_both(B);
        STAMP(25);
        {
            if constexpr (!SPARE) {
                const u32x4 rw2 = mkrsrc(A.w2, 1792 * 896); const u64 t0 = ksel(wv, 0);
                const unsigned v = (lane2 >> 3) * 896 + (((lane2 ^ (lane2 >> 3 & 6)) & 7) << 4) + A.cb * 128, s0 = ((unsigned)t0 & 63u) * (32 * 896), s1_ = ((unsigned)(t0 >> 8) & 63u) * (32 * 896);
                dma16<L_RING>(v, rw2, s0, wv * UNIT); dma16<L_RING + 1024>(v + 7168, rw2, s0, wv * UNIT); dma16<L_RING + 2048>(v + 2 * 7168, rw2, s0, wv * UNIT); dma16<L_RING + 3072>(v + 3 * 7168, rw2, s0, wv * UNIT);
                dma16<L_RING + 4096>(v, rw2, s1_, wv * UNIT); dma16<L_RING + 5120>(v + 7168, rw2, s1_, wv * UNIT); dma16<L_RING + 6144>(v + 2 * 7168, rw2, s1_, wv * UNIT); dma16<L_RING + 7168>(v + 3 * 7168, rw2, s1_, wv * UNIT);
            } else {
                const unsigned v0 = P.v2 + (27 - A.fb2) * 128, v1 = v0 + 32 * 3584;
#define X27(i) do { const unsigned so = wrap7(P.c0 + i) * (128 * 1792); dma16<L_RING + i * UNIT>(v0, P.rw, so, P.wk); dma16<L_RING + i * UNIT + 4096>(v1, P.rw, so, P.wk); } while (0)
                X27(0); X27(1); X27(2); X27(3); X27(4);
#undef X27
            }
        }
        {
            const u32x4 rQ = mkrsrc(A.ws + WS_Q + hd * (64 * 1792), (4 * art + 4) * 1792), rK = mkrsrc(A.ws + WS_K + hd * (64 * 1792), (4 * art + 4) * 1792), rV = mkrsrc(A.ws + WS_V + hd * (64 * 2048), 64 * 2048);
            unsigned kv0 = tid2 * 16 + (4 * art - 8) * 1792, vv0 = tid2 * 16 + (4 * art - 8) * 2048;
            PINV(kv0); PINV(vv0);
            *(LAS u32x4*)(size_t)(L_V + min(lane2, 11u) * 2048 + 28 * 16) = ones4;
            if (wv == 0) {
                const unsigned ln = min(lane2, 13u), sel = ln >= 7, m = ln - 7 * sel, sec = (m >= 1) + (m >= 4);
                const int t = (int)(art + m) - 3 * (int)sec;
                __builtin_amdgcn_s_sleep(ESLP);
                spin_flags(rs, WS_FQ + ((t < 0 ? art : t) * 21 + ((448 * sec + 28 * hd + 27 * sel) >> 6)) * 128, ep);
                lds_arrive(L_CNT + 36);
            } else if (!SPARE && wv == 1) {
                __builtin_amdgcn_s_sleep(ESLP);
                spin_flags(rs, WS_FH + ((rt64 >> 1) * 28 + 5 + 2 * min(lane2, 10u)) * 128, ep);
            }
            lds_wait(L_CNT + 36, 1);
            STAMP(28);
            dma16c<L_Q>(kv0 + 8 * 1792, rQ, P.wk); dma16c<L_Q + 4096>(kv0 + 8 * 1792 + 4096, rQ, P.wk);
            dma16c<L_K>(kv0, rK, P.wk); dma16c<L_K + 4096>(kv0 + 4096, rK, P.wk); dma16c<L_K + 2 * 4096>(kv0 + 2 * 4096, rK, P.wk);
            dma16c<L_K + 3 * 4096>(kv0 + 3 * 4096, rK, P.wk); dma16c<L_K + 4 * 4096>(kv0 + 4 * 4096, rK, P.wk); dma16c<L_K + 5 * 4096>(kv0 + 5 * 4096, rK, P.wk);
            WAITV(8); lds_arrive(L_CNT + 60);
            dma16v<L_V>(vv0, rV, P.wk); dma16v<L_V + 4096>(vv0 + 4096, rV, P.wk); dma16v<L_V + 2 * 4096>(vv0 + 2 * 4096, rV, P.wk);
            dma16v<L_V + 3 * 4096>(vv0 + 3 * 4096, rV, P.wk); dma16v<L_V + 4 * 4096>(vv0 + 4 * 4096, rV, P.wk); dma16v<L_V + 5 * 4096>(vv0 + 5 * 4096, rV, P.wk);
            __builtin_amdgcn_s_setprio(PRIO_A);
            STAMP(29);
        }
        unsigned ka0 = L_K + wv * 1792 + p4b * 448 + (8 * gb + q4b) * 16 + ((gb & 1) << 3), ka1 = ka0 ^ 8;
        unsigned va0 = L_V + wv * 2048 + gb * 512 + r16b * 16, va1 = L_V + wv * 2048 + vsel(p4b == 3, 28 * 16, gb * 512 + (16 + 3 * q4b + p4b) * 16);
        unsigned ost = WS_O + ((8 * art + 2 * wv) * 16 + hd) * 1024 + lane2 * 16;
        f32x4 cml, cmh;
#pragma unroll
        for (int j = 0; j < 4; ++j) { const bool newer = 4 * gb + j > r16b; cml[j] = newer ? 0.f : -INFINITY; cmh[j] = newer ? -INFINITY : 0.f; }
        PINV(ka0); PINV(ka1); PINV(va0); PINV(va1); PINV(ost); PINV(cml); PINV(cmh);
        WAITV(6); lds_arrive(L_CNT); lds_wait(L_CNT, 4);
        STAMP(5);
        {
            HalfFrag qh[2], kf[18];
            rd_tr(qh[0], ka0 - (L_K - L_Q), 0, 64); rd_tr(qh[1], ka1 - (L_K - L_Q), 0, 64);
            f32x4 s[2][18];
            bf16x8 vf[9][2];
            float m[2] = {-INFINITY, -INFINITY};
#define MX(u, t) do { m[u] = __builtin_fmaxf(__builtin_fmaxf(m[u], s[u][t][0]), s[u][t][1]); m[u] = __builtin_fmaxf(__builtin_fmaxf(m[u], s[u][t][2]), s[u][t][3]); PINV(m[u]); } while (0)
            if (art >= 2) {
#pragma unroll
                for (int t = 0; t < 3; ++t) rd_tr(kf[t], (t & 1) ? ka1 : ka0, (t >> 1) * 1792, 64);
                if (gb == 3) { qh[0].hi = (s16x4){0, 0, 0, 0}; qh[1].hi = (s16x4){0, 0, 0, 0}; }
                const bf16x8 qf0 = join(qh[0]), qf1 = join(qh[1]);
#pragma unroll
                for (int t = 0; t < 18; ++t) {
                    if (t < 17) s[0][t] = MFMA(join(kf[t]), qf0, t == 0 ? cml : (t == 16 ? cmh : ZERO4));
                    if (t + 3 < 18) rd_tr(kf[t + 3], (t & 1) ? ka0 : ka1, ((t + 3) >> 1) * 1792, 64);
                    if (t >= 2) MX(0, t - 2);
                    SB();
                    if (t > 0) s[1][t] = MFMA(join(kf[t]), qf1, t == 1 ? cml : (t == 17 ? cmh : ZERO4));
                    if (t >= 3) MX(1, t - 2);
                    if (t == 10) { WAITV(0); lds_arrive(L_CNT + 44); }
                    SB();
                }
                MX(0, 16); MX(1, 16); MX(1, 17);
            } else {
                const int b0 = 8 - 4 * (int)art - (int)wv;
                WAITV(0); lds_arrive(L_CNT + 44);
                if (gb == 3) { qh[0].hi = (s16x4){0, 0, 0, 0}; qh[1].hi = (s16x4){0, 0, 0, 0}; }
                const bf16x8 qf0 = join(qh[0]), qf1 = join(qh[1]);
                const f32x4 ninf = {-INFINITY, -INFINITY, -INFINITY, -INFINITY};
#pragma unroll
                for (int b = 0; b < 9; ++b) {
                    if (b >= b0) {
                        rd_tr(kf[0], ka0, b * 1792, 64); rd_tr(kf[1], ka1, b * 1792, 64);
                        s[0][2 * b] = MFMA(join(kf[0]), qf0, b == 8 ? cmh : ZERO4); s[1][2 * b] = MFMA(join(kf[0]), qf1, ZERO4);
                        s[0][2 * b + 1] = MFMA(join(kf[1]), qf0, ZERO4); s[1][2 * b + 1] = MFMA(join(kf[1]), qf1, b == 8 ? cmh : ZERO4);
                        MX(0, 2 * b); MX(1, 2 * b); if (b < 8) MX(0, 2 * b + 1); MX(1, 2 * b + 1);
                    } else { s[0][2 * b] = ninf; s[1][2 * b] = ninf; s[0][2 * b + 1] = ninf; s[1][2 * b + 1] = ninf; }
                }
            }
#undef MX
            if constexpr (!SPARE) lds_arrive(L_CNT + 4);
            const float SC = 0.27264391f;
            const float mc[2] = {-SC * redmax4(m[0]), -SC * redmax4(m[1])};
            lds_wait(L_CNT + 44, 4);
#pragma unroll
            for (int b = 0; b < 2; ++b) { vf[b][0] = lds128(va0 + b * 2048); vf[b][1] = lds128(va1 + b * 2048); }
            STAMP(6);
            f32x4 o[2][2] = {{ZERO4, ZERO4}, {ZERO4, ZERO4}};
            u32x4 pw[2];
#pragma unroll
            for (int i = 0; i < 19; ++i) {
                const int b = i >> 1, u = i & 1, pb = (i - 1) >> 1, pu = (i - 1) & 1;
                const bool lo = i < 18 && (u == 0 || b > 0), hi = i < 18 && (u == 1 || b < 8);
                f32x4 ea = ZERO4, eb = ZERO4;
                if (lo) ea = s[u][2 * b] * SC + mc[u];
                if (hi) eb = s[u][2 * b + 1] * SC + mc[u];
                if (u == 0 && b + 2 < 9) { vf[b + 2][0] = lds128(va0 + (b + 2) * 2048); vf[b + 2][1] = lds128(va1 + (b + 2) * 2048); }
                SB();
                if (lo) { ea[0] = __builtin_amdgcn_exp2f(ea[0]); ea[1] = __builtin_amdgcn_exp2f(ea[1]); PINV(ea); }
                SB();
                if (i > 0) o[pu][0] = MFMA(vf[pb][0], asf(pw[pu]), o[pu][0]);
                if (lo) { ea[2] = __builtin_amdgcn_exp2f(ea[2]); ea[3] = __builtin_amdgcn_exp2f(ea[3]); PINV(ea); }
                if (hi) { eb[0] = __builtin_amdgcn_exp2f(eb[0]); eb[1] = __builtin_amdgcn_exp2f(eb[1]); PINV(eb); }
                SB();
                if (i > 0) o[pu][1] = MFMA(vf[pb][1], asf(pw[pu]), o[pu][1]);
                if (hi) { eb[2] = __builtin_amdgcn_exp2f(eb[2]); eb[3] = __builtin_amdgcn_exp2f(eb[3]); PINV(eb); }
                SB();
                if (i < 18) { pw[u] = (u32x4){lo ? pk2(ea[0], ea[1]) : 0u, lo ? pk2(ea[2], ea[3]) : 0u, hi ? pk2(eb[0], eb[1]) : 0u, hi ? pk2(eb[2], eb[3]) : 0u}; PINV(pw[u]); }
                SB();
            }
#pragma unroll
            for (int u = 0; u < 2; ++u) {
                const float il = __builtin_amdgcn_rcpf(o[u][1][3]);
                __builtin_amdgcn_raw_buffer_store_b128((u32x4){pk2(o[u][0][0] * il, o[u][0][1] * il), pk2(o[u][0][2] * il, o[u][0][3] * il), pk2(o[u][1][0] * il, o[u][1][1] * il), pk2(o[u][1][2] * il, 0.f)}, rs, ost + u * 16384, 0, 16);
            }
        }
        STAMP(7);
        const unsigned fo = WS_FO + (art * 16 + hd) * 128 + A.l0;
        WAITV(0);
        put_flag(rs, fo | last_of(L_CNT + 32, 4), ep);
        __builtin_amdgcn_s_setprio(0);
        if constexpr (SPARE) {
            if (A.last && wv == 0) {
                __builtin_amdgcn_s_sleep(64);
#pragma nounroll
                for (unsigned it = 0; it < SPIN_MAX; ++it) {
                    unsigned bad = 0;
                    asm volatile("" ::: "memory");
#pragma unroll
                    for (int i = 0; i < 4; ++i) { const u32x2 f = get_flag(rs, WS_FO + (i * 64 + lane2) * 128); bad |= (f[0] ^ ep[0]) | (f[1] ^ ep[1]); }
                    if (__builtin_amdgcn_ballot_w64(bad != 0) == 0) break;
                    __builtin_amdgcn_s_sleep(64);
                }
                put_flag(rs, WS_E + A.l0, ep);
            }
            asm volatile(".Lfk_end:");
            FIN();
            __builtin_amdgcn_endpgm();
        } else {
            xres = *(const GAS u32x4*)(A.x + oxo);
            const unsigned d0 = 4 * gb + q4b, d1 = 16 + 3 * gb + min(q4b, 2u);
            unsigned a0[4], a1[4], a2[4], a3[4];
#pragma unroll
            for (unsigned nt = 0; nt < 4; ++nt) {
                a0[nt] = L_WO + d0 * 128 + ((nt ^ ((d0 >> 1) & 3)) << 5) + p4b * 8; a1[nt] = L_WO + d1 * 128 + ((nt ^ ((d1 >> 1) & 3)) << 5) + p4b * 8;
                a2[nt] = a0[nt] + L_WO2; a3[nt] = a1[nt] + L_WO2;
                PINV(a0[nt]); PINV(a1[nt]); PINV(a2[nt]); PINV(a3[nt]);
            }
            const unsigned foff = WS_FO + ((rt64 >> 1) * 16 + r16b) * 128, obase = WS_O + (4 * rt64 + wv) * 16384 + lane2 * 16;
            HalfFrag wof[16][4];
#define RWO(i) do { constexpr int i_ = (i) < 9 ? (i) : (i) - 9, o_ = i_ * 3584; _Pragma("unroll") for (int nt = 0; nt < 4; ++nt) { \
                wof[i][nt].lo = ldstr(((i) < 9 ? a0 : a2)[nt ^ ((i_ & 1) << 1)] + o_); wof[i][nt].hi = ldstr(((i) < 9 ? a1 : a3)[nt ^ ((i_ & 1) << 1)] + o_); } } while (0)
            lds_wait(L_CNT + 8, 4);
            STAMP(30);
            RWO(0); RWO(1); RWO(2); RWO(3); RWO(4); RWO(5);
            SB();
            if (wv == 0) { __builtin_amdgcn_s_sleep(OSLP); spin_flags(rs, foff, ep); lds_arrive(L_CNT + 12); } else lds_wait(L_CNT + 12, 1);
            STAMP(9);
            u32x4 of[16];
#pragma unroll
            for (int i = 0; i < 16; ++i) of[i] = __builtin_amdgcn_raw_buffer_load_b128(rs, obase + (i & 3) * 1024, (i >> 2) * 4096, 16);
            SB();
            f32x4 ac[4] = {ZERO4, ZERO4, ZERO4, ZERO4};
#define MWO(i) do { _Pragma("unroll") for (int nt = 0; nt < 4; ++nt) ac[nt] = MFMA(join(wof[i][nt]), asf(of[i]), ac[nt]); } while (0)
            MWO(0); RWO(6); SB();
            MWO(1); RWO(7); SB();
            MWO(2); RWO(8); SB();
            MWO(3); RWO(9); SB();
            MWO(4); RWO(10); SB();
            MWO(5); RWO(11); SB();
            MWO(6); RWO(12); SB();
            MWO(7); RWO(13); SB();
            MWO(8); RWO(14); SB();
            MWO(9); RWO(15); SB();
            MWO(10); SB();
            MWO(11); SB();
            MWO(12); SB();
            MWO(13); SB();
            MWO(14); SB();
            MWO(15); SB();
#undef MWO
#undef RWO
            STAMP(8);
            BAR();
#pragma unroll
            for (int nt = 0; nt < 4; ++nt) *(LAS f32x4*)(size_t)(L_RING + (8 + wv) * UNIT + r16b * 256 + (((nt * 4 + gb) ^ r16b) << 4)) = ac[nt];
            final_sum(A, xres DIAGARG);
        }
    }
}

__global__ void __launch_bounds__(512, 2) fused_k(const char* __restrict__ x, const char* __restrict__ wqkv, const char* __restrict__ wo, const char* __restrict__ w1,
                                                  const char* __restrict__ b1, const char* __restrict__ w2, const char* __restrict__ b2, char* __restrict__ out, char* ws) {
    STAMP(0);
    const unsigned tid = threadIdx.x, lane = tid & 63, wv = __builtin_amdgcn_readfirstlane(tid >> 6);
    const unsigned bid = blockIdx.x, xcd = bid & 7, idx = bid >> 3;
    {
        u64 pc; unsigned len;
        asm volatile("s_getpc_b64 %0\n.Lfk_begin:\n\ts_mov_b32 %1, .Lfk_end-.Lfk_begin-256" : "=s"(pc), "=s"(len));
        gdma4(lane * 4, (const char*)(pc + min(idx * 1664 + wv * 256, len)), L_JUNK + wv * 256);
    }
    const unsigned rt = (xcd & 1) * 8 + (idx & 7), G = xcd >> 1, slot = G * 4 + (idx >> 3);
    const bool two = slot < 5;
    const unsigned qb0 = two ? 2 * slot : slot + 5;
    const unsigned blk1 = two ? 2 * slot + 1 : 2 * slot - 5;
    const unsigned fb2 = two ? slot : 2 * slot - 4;
    const unsigned drow = tid >> 3, dc = lane & 7;
    P1 P;
    P.wk = wv * 1024; P.c0 = (rt + slot) - 7 * (((rt + slot) * 37) >> 8); P.lane = lane;
    P.xv0 = (rt * 128 + drow) * 896 + ((dc ^ ((drow >> 1) & 7)) << 4); P.xv1 = P.xv0 + 64 * 896;
    const unsigned wsw1 = (dc ^ ((drow & 2) | ((drow >> 1) & 4))) << 4;
    P.v0 = drow * 2688 + wsw1 + qb0 * 128; P.v2 = drow * 3584 + wsw1;
    P.m1 = two ? 1344 : 1792; P.v1 = drow * 2 * P.m1 + wsw1 + blk1 * 128;
    const unsigned srow_off = (rt * 128 + slot * 8 + wv) * 896 + lane * 16 + (lane < 56 ? 0 : OOB);
    asm volatile("" : "+s"(x), "+s"(wqkv), "+s"(wo), "+s"(w1), "+s"(b1), "+s"(w2), "+s"(b2), "+s"(out), "+s"(ws));
    P.rx = mkrsrc(x, 2048 * 896); P.rq = mkrsrc(wqkv, 448 * 2688); P.rw = mkrsrc(w1, 448 * 3584); P.r1 = two ? P.rq : P.rw;
    const u64 e0 = *(const __attribute__((address_space(4))) u64*)(ws + WS_E);
    asm volatile("buffer_load_dwordx4 %0, %1, %2, 0 offen" : "=v"(P.srow) : "v"(srow_off), "s"(P.rx) : "memory");
    piece<0>(P); piece<1>(P); piece<2>(P); piece<3>(P);
    const bool spare = idx - (G == 0 ? 24u : 28u) < 4u;
    const bool extra = spare && G < 2;
    const unsigned p3 = spare ? 0 : (idx >= 28 ? idx - 4 : idx);
    const unsigned p37 = (p3 * 37) >> 8, rt64 = 4 * xcd + p37, cb = p3 - 7 * p37;
    {
        const unsigned w3 = wv & 3, sel = 27u | (fb2 << 8) | ((blk1 & 31) << 16) | (cb << 24);
        gdma4((lane & 31) * 4 + ((sel >> (8 * w3)) & 31) * 128, w3 == 3 ? b2 : b1, L_B1 + w3 * 256 + (wv >> 2) * 4096);
    }
    if constexpr (A_PRE > 4) piece<4>(P); if constexpr (A_PRE > 5) piece<5>(P); if constexpr (A_PRE > 6) piece<6>(P); if constexpr (A_PRE > 7) piece<7>(P);
    {
        const unsigned k = bid * 8 + wv, k3 = min(k, 2015u) - 1960;
        gdma4(k < 1960 ? k * 1024 + (lane & 7) * 128 : (k3 * 8 + (lane & 7)) * 3584, k < 1568 ? w2 : (k < 1960 ? wo - 1568 * 1024 : w1 + 27 * 128), L_JUNK + wv * 256);
    }
    P.v2 += fb2 * 128;
    STAMP(12);
    const unsigned r16 = lane & 15, g = lane >> 4, q4 = r16 >> 2, p4 = lane & 3;
    const unsigned slab = wv & 3, ch = wv >> 2, s1 = slab >> 1;
    const u64 ep64 = e0 * 0xD1342543DE82EF95ull + 0x9E3779B97F4A7C15ull;
    const u32x2 ep = {(unsigned)ep64, (unsigned)(ep64 >> 32)};
    const rsrc_t rs = __builtin_amdgcn_make_buffer_rsrc(ws, 0, (int)WS_END, 0x00020000);
    {
        const unsigned fl = r16 >> 1, key = ((q4 >> 1) & 1) | ((g & 1) << 1);
#pragma unroll
        for (unsigned e = 0; e < 2; ++e) {
            const unsigned ee = e ^ ch ^ (slab & 1);
            P.xa[e] = (slab >> 1) * UNIT + ((slab & 1) * 32 + r16) * 128 + (((4 * ee + g) ^ fl) << 4);
#pragma unroll
            for (unsigned nt = 0; nt < 2; ++nt) P.wa[e][nt] = ee * 4096 + (8 * g + q4) * 128 + (((2 * ch + (nt ^ s1)) ^ key) << 5) + p4 * 8;
        }
    }
    const unsigned tb = 4 * rt + slab;
    P.rs = rs; P.tag = ep[1]; P.gb = WS_GS + tb * 256; P.gs = WS_GS + (rt * 128 + slot * 8 + wv) * 8;
    unsigned qd[2][2];
#pragma unroll
    for (unsigned nt = 0; nt < 2; ++nt) {
        const unsigned nl = 32 * ch + 16 * (nt ^ s1);
        qd[0][nt] = qkv_dest(64 * qb0 + nl + r16, tb, g); qd[1][nt] = qkv_dest(64 * (two ? blk1 : qb0) + nl + r16, tb, g);
    }
    u32x4 ones4 = {0x3f803f80u, 0x3f803f80u, 0x3f803f80u, 0x3f803f80u}; PINV(ones4);
    const bf16x8 ones = asf(ones4);
    *(LAS u32x4*)(size_t)(L_CNT + (lane & 3) * 16) = (u32x4){0u, 0u, 0u, 0u};
    bf16x8 xf[14][2];
    f32x4 acc[2][2][2];
#pragma unroll
    for (int i = 0; i < 8; ++i) acc[i >> 2][(i >> 1) & 1][i & 1] = ZERO4;
    Epi E; E.csw = L_CS + (slab & 1) * 256 + (32 * ch + 16 * s1) * 4; E.s1 = s1;
    asm volatile("s_waitcnt vmcnt(%1)" : "+v"(P.srow) : "i"(A_PRE + 2) : "memory");
    stats_stage<0>(P);
    WAITBAR(A_PRE - 2);
    if (two) pass_a<true>(xf, acc, ones, P, E DIAGARG);
    else pass_a<false>(xf, acc, ones, P, E DIAGARG);
    STAMP(1);
    __builtin_amdgcn_s_setprio(2);
    epi_qkv(acc[0], E.mq, E.rq, E.csq[0], qd[0], rs);
    if (two) epi_qkv(acc[1], E.mq, E.rq, E.csq[1], qd[1], rs);
    if (wv < 4) __builtin_amdgcn_s_setprio(0); else __builtin_amdgcn_s_setprio(1);
    STAMP(2);
    unsigned tid1 = threadIdx.x; PINV(tid1);
    const unsigned hst = WS_H + 2 * tb * 56 * 1024 + (tid1 & 63) * 16, l0 = (tid1 & 63) == 0 ? 0 : OOB;
    const unsigned fhw = WS_FH + rt * 28 * 128 + l0;
    PB B; B.rs = rs; B.ep = ep; B.two = two; B.rws = mkrsrc(ws, WS_END);
    B.f0 = WS_FQ + (rt * 21 + qb0) * 128 + l0; B.f1 = two ? B.f0 + 128 : fhw + blk1 * 128;
    if (!two) {
        const unsigned ta[2] = {64 * ch + 32 * s1 + ((tid1 & 48) >> 1), 64 * ch + 32 * (s1 ^ 1) + ((tid1 & 48) >> 1)};
        u32x4 od[2]; epi_ffn(acc[1], od, ta, L_CS + 512, 2, s1, E.mr, E.rstd);
#pragma unroll
        for (int tt = 0; tt < 2; ++tt) __builtin_amdgcn_raw_buffer_store_b128(od[tt], rs, hst + (2 * blk1 + ch) * 1024 + tt * 56 * 1024, 0, 16);
    }
    STAMP(15);
    Rest A; A.x = x; A.wo = wo; A.w2 = w2; A.out = out; A.ws = ws; A.wv = wv; A.hd = 2 * xcd + (idx & 1); A.art = idx >> 1; A.rt64 = rt64; A.cb = cb; A.fb2 = fb2; A.s1 = s1;
    A.hst = hst; A.fhw = fhw; A.l0 = l0; A.extra = extra; A.last = bid == 4 + 8 * 28;
    if (spare) { rest<true>(xf, ones4, P, B, E, A DIAGARG); __builtin_unreachable(); }
    rest<false>(xf, ones4, P, B, E, A DIAGARG);
}

extern "C" void kernel_launch(void* const* d_in, const int* in_sizes, int n_in, void* d_out, int out_size, void* d_ws, size_t ws_size, hipStream_t stream) {
    (void)in_sizes; (void)n_in; (void)out_size; (void)ws_size;
    (void)hipFuncSetAttribute((const void*)fused_k, hipFuncAttributeMaxDynamicSharedMemorySize, LDS_BYTES);
    fused_k<<<256, 512, LDS_BYTES, stream>>>((const char*)d_in[0], (const char*)d_in[1], (const char*)d_in[2], (const char*)d_in[4], (const char*)d_in[5],
                                             (const char*)d_in[6], (const char*)d_in[7], (char*)d_out, (char*)d_ws);
}
```

```cpp
#include <hip/hip_runtime.h>
#include <stdint.h>

typedef unsigned long long u64;
typedef short bf16x8 __attribute__((ext_vector_type(8)));
typedef short s16x4 __attribute__((ext_vector_type(4)));
typedef __bf16 b16x2 __attribute__((ext_vector_type(2)));
typedef float f32x4 __attribute__((ext_vector_type(4)));
typedef float f32x2 __attribute__((ext_vector_type(2)));
typedef unsigned u32x4 __attribute__((ext_vector_type(4)));
typedef unsigned u32x2 __attribute__((ext_vector_type(2)));
typedef __amdgpu_buffer_rsrc_t rsrc_t;
#define LAS __attribute__((address_space(3)))
#define GAS __attribute__((address_space(1)))
#define DI __device__ __forceinline__
#define RLX_AGENT __ATOMIC_RELAXED, __HIP_MEMORY_SCOPE_AGENT

constexpr unsigned WS_E = 0;
constexpr unsigned WS_FQ = 4096;
constexpr unsigned WS_FH = 49152;
constexpr unsigned WS_FO = 106496;
constexpr unsigned WS_GS = 139264;
constexpr unsigned WS_Q = 163840;
constexpr unsigned WS_K = WS_Q + 16 * 64 * 1792;
constexpr unsigned WS_V = WS_K + 16 * 64 * 1792;
constexpr unsigned WS_H = WS_V + 16 * 64 * 2048;
constexpr unsigned WS_O = WS_H + 128 * 56 * 1024;
constexpr unsigned WS_END = WS_O + 128 * 16 * 1024;

constexpr int UNIT = 8192;
constexpr int phys(int piece) { return (((piece % 19) + 3) % 19) * UNIT; }
constexpr int L_REG = 12 * UNIT;
constexpr int L_Q = 0, L_K = UNIT, L_V = 4 * UNIT, L_WO = 0, L_WO2 = 15 * UNIT;
constexpr int L_TB = 19 * UNIT;
constexpr int L_CS = L_TB, L_B1 = L_TB + 1024, L_B2 = L_B1 + 768, L_JUNK = L_TB + 2048, L_ST = L_TB + 6144;
constexpr int L_RING = 7 * UNIT, L_CNT = L_TB + 4096, L_CS2 = L_TB + 4608;
constexpr int rslot(int ks) { return ((ks & 3) >> 1) * 4 * UNIT + (ks & 1) * 4096; }
constexpr int LDS_BYTES = 163840;

#define BD0 2
#define HF1_AT 6
#define POLL0 11
constexpr int b_upto(int mode, int p) { int c = 0; for (int s = 0; s <= p; ++s) c += (s >= BD0 && s - BD0 < (mode ? 0 : 4)); return c; }
#define SLP 4
#define OSLP 24
constexpr unsigned SPIN_MAX = 400000;
constexpr unsigned OOB = 0x80000000u;

#define MFMA(a, b, c) __builtin_amdgcn_mfma_f32_16x16x32_bf16((a), (b), (c), 0, 0, 0)
#define WAITBAR(N) asm volatile("s_waitcnt vmcnt(%0) lgkmcnt(0)\n\ts_barrier" ::"i"(N) : "memory")
#define BAR() asm volatile("s_waitcnt lgkmcnt(0)\n\ts_barrier" ::: "memory")
#define WAITV(N) asm volatile("s_waitcnt vmcnt(%0)" ::"i"(N) : "memory")
#define SB() __builtin_amdgcn_sched_barrier(0)
#define PINV(x) asm volatile("" : "+v"(x))
#define PINS(x) asm volatile("" : "+s"(x))
#define ZERO4 ((f32x4){0.f, 0.f, 0.f, 0.f})

DI u32x4 mkrsrc(const char* p, unsigned bytes) { const u64 a = (u64)p; return (u32x4){(unsigned)a, (unsigned)(a >> 32), bytes, 0x20000u}; }
template <int DST> DI void dma16(unsigned voff, u32x4 r, unsigned soff, unsigned wk) {
    asm volatile("s_add_u32 m0, %3, %4\n\ts_nop 0\n\tbuffer_load_dwordx4 %0, %1, %2 offen lds" ::"v"(voff), "s"(r), "s"(soff), "s"(wk), "i"(DST) : "memory", "scc");
}
template <int DST, int SOFF> DI void dma16i(unsigned voff, u32x4 r, unsigned wk) {
    unsigned t;
    asm volatile("s_add_u32 m0, %3, %4\n\ts_mov_b32 %0, %5\n\tbuffer_load_dwordx4 %1, %2, %0 offen lds" : "=&s"(t) : "v"(voff), "s"(r), "s"(wk), "i"(DST), "i"(SOFF) : "memory", "scc");
}
template <int DST> DI void dma16c(unsigned voff, u32x4 r, unsigned wk) {
    asm volatile("s_add_u32 m0, %2, %3\n\ts_nop 0\n\tbuffer_load_dwordx4 %0, %1, 0 offen sc1 lds" ::"v"(voff), "s"(r), "s"(wk), "i"(DST) : "memory", "scc");
}
template <int DST> DI void dma16v(unsigned voff, u32x4 r, unsigned wk) {
    asm volatile("s_add_u32 m0, %2, %3\n\ts_mov_b32 exec_lo, 0x0fffffff\n\ts_mov_b32 exec_hi, 0x0fffffff\n\tbuffer_load_dwordx4 %0, %1, 0 offen sc1 lds\n\ts_mov_b64 exec, -1" ::"v"(voff), "s"(r), "s"(wk), "i"(DST) : "memory", "scc");
}
DI void gdma4(unsigned voff, const char* sbase, unsigned ldsdst) {
    asm volatile("s_mov_b32 m0, %2\n\ts_nop 0\n\tglobal_load_lds_dword %0, %1" ::"v"(voff), "s"(sbase), "s"(ldsdst) : "memory");
}
DI bf16x8 lds128(unsigned a) { return *(const LAS bf16x8*)(size_t)a; }
DI f32x4 ldsf4(unsigned a) { return *(const LAS f32x4*)(size_t)a; }
DI float ldsf(unsigned a) { return *(const LAS float*)(size_t)a; }
DI s16x4 ldstr(unsigned a) { return __builtin_amdgcn_ds_read_tr16_b64_v4i16((LAS s16x4*)(size_t)a); }
DI unsigned pk2(float a, float b) { f32x2 p = {a, b}; return __builtin_bit_cast(unsigned, __builtin_convertvector(p, b16x2)); }
DI float rbf(float a) { return __uint_as_float(pk2(a, 0.f) << 16); }
DI float bflo(unsigned u) { return __uint_as_float(u << 16); }
DI float bfhi(unsigned u) { return __uint_as_float(u & 0xffff0000u); }
DI bf16x8 asf(u32x4 v) { return __builtin_bit_cast(bf16x8, v); }
DI float redmax4(float v) {
    auto a = __builtin_amdgcn_permlane16_swap(__float_as_uint(v), __float_as_uint(v), false, false); v = __builtin_fmaxf(__uint_as_float(a[0]), __uint_as_float(a[1]));
    auto b = __builtin_amdgcn_permlane32_swap(__float_as_uint(v), __float_as_uint(v), false, false); return __builtin_fmaxf(__uint_as_float(b[0]), __uint_as_float(b[1])); }
template <int CTRL> DI float dppadd(float v) { return v + __uint_as_float(__builtin_amdgcn_update_dpp(0, __float_as_uint(v), CTRL, 0xf, 0xf, true)); }
DI float wavesum(float v) {
    v = dppadd<0xB1>(v); v = dppadd<0x4E>(v); v = dppadd<0x141>(v); v = dppadd<0x140>(v);
    auto a = __builtin_amdgcn_permlane16_swap(__float_as_uint(v), __float_as_uint(v), false, false); v = __uint_as_float(a[0]) + __uint_as_float(a[1]);
    auto b = __builtin_amdgcn_permlane32_swap(__float_as_uint(v), __float_as_uint(v), false, false); return __uint_as_float(b[0]) + __uint_as_float(b[1]); }
DI void put_flag(rsrc_t rs, unsigned off, u32x2 v) { __builtin_amdgcn_raw_buffer_store_b64(v, rs, off, 0, 16); }
DI u32x2 get_flag(rsrc_t rs, unsigned off) { return __builtin_amdgcn_raw_buffer_load_b64(rs, off, 0, 16); }
DI unsigned last_of(unsigned cnt, unsigned n) {
    unsigned r;
    asm volatile("s_mov_b64 exec, 1\n\tds_add_rtn_u32 %0, %1, %2\n\ts_mov_b64 exec, -1\n\ts_waitcnt lgkmcnt(0)" : "=&v"(r) : "v"(cnt), "v"(1u) : "memory");
    return (unsigned)__builtin_amdgcn_readfirstlane(r) == n - 1 ? 0u : 0x80000000u;
}
DI bool all_set(u32x2 f, u32x2 ep) { return __builtin_amdgcn_ballot_w64(((f[0] ^ ep[0]) | (f[1] ^ ep[1])) != 0) == 0; }
DI void spin_flags(rsrc_t rs, unsigned off, u32x2 ep) {
#pragma nounroll
    for (unsigned it = 0; it < SPIN_MAX; ++it) {
        asm volatile("" ::: "memory");
        if (all_set(get_flag(rs, off), ep)) break;
        __builtin_amdgcn_s_sleep(SLP);
    }
    asm volatile("" ::: "memory");
}
DI void lds_arrive(unsigned a) {
    asm volatile("s_waitcnt lgkmcnt(0)\n\ts_mov_b64 exec, 1\n\tds_add_u32 %0, %1\n\ts_mov_b64 exec, -1" ::"v"(a), "v"(1u) : "memory");
}
DI void lds_wait(unsigned a, unsigned n) {
#pragma nounroll
    for (unsigned it = 0; it < SPIN_MAX; ++it) {
        asm volatile("" ::: "memory");
        if (__builtin_amdgcn_readfirstlane(__hip_atomic_load((LAS unsigned*)(size_t)a, __ATOMIC_RELAXED, __HIP_MEMORY_SCOPE_WORKGROUP)) >= n) break;
        __builtin_amdgcn_s_sleep(2);
    }
    asm volatile("" ::: "memory");
}
DI unsigned vsel(bool c, unsigned a, unsigned b) { PINV(a); PINV(b); return c ? a : b; }
DI unsigned wrap7(unsigned v) { return v >= 7 ? v - 7 : v; }
constexpr int kblk(int f, int p) { return f == 3 ? (p < 2 ? 23 + 2 * p : (p < 6 ? 16 + 2 * p : 27)) : (p < 3 ? 5 + 6 * f + 2 * p : (f == 0 ? p - 3 : (f == 1 && p == 3 ? 4 : 8 * f + 2 * p - 10))); }
constexpr unsigned kword(int f, int p0, int n) { unsigned v = 0; for (int p = 0; p < n; ++p) v |= (unsigned)kblk(f, p0 + p) << (8 * p); return v; }
DI u64 ktable(unsigned f, unsigned r3, unsigned r4) {
    const unsigned late = f == 0 ? kword(0, 3, 4) : (f == 1 ? kword(1, 3, 4) : (f == 2 ? kword(2, 3, 4) : kword(3, 2, 4)));
    const u64 lr = (unsigned)((late | (u64)late << 32) >> (8 * r4));
    const unsigned e3 = kword(0, 0, 3) + f * 0x060606u;
    return f == 3 ? (((r4 & 1) ? 0x1719u : 0x1917u) | lr << 16 | 27ull << 48) : ((((e3 | (u64)e3 << 24) >> (8 * r3)) & 0xffffffu) | lr << 24);
}
#define ROT 1
#define WO0 3
#define WOS 6
#define PRIO_A 3
#define PRIO_F 0
constexpr int fw(int s) { return s + 3 < 14 ? s + 3 : -1; }
constexpr int fh(int s, int j) { return s < 2 ? (j ? -1 : s + 2) : (s == 2 ? 4 + j : (s == 3 ? 6 + j : (j == 0 && s + 4 < 14 ? s + 4 : -1))); }
constexpr int f_nw(int s) { return (s >= WO0 && s < WO0 + 3) ? 4 : (s == WO0 + 3 ? 3 : 0); }
constexpr int f_np(int s) { return (s == 0 ? 1 : 0) + (s == 6 ? 1 : 0) + (s == 10 ? 1 : 0); }
constexpr int f_all(int s) { return 4 * ((fw(s) >= 0) + (fh(s, 0) >= 0) + (fh(s, 1) >= 0)) + f_nw(s) + f_np(s); }
constexpr int f_cum(int s) { int c = 13; for (int i = 0; i < s; ++i) c += f_all(i); return c; }
constexpr int f_wend(int k) { return k == 2 ? 12 : f_cum(k - 3) + 4; }
constexpr int f_hend(int k) {
    if (k < 2) return 4 + 4 * k;
    for (int s = 0; s < 14; ++s) { const int w = 4 * (fw(s) >= 0); if (fh(s, 0) == k) return f_cum(s) + w + 4; if (fh(s, 1) == k) return f_cum(s) + w + 8; }
    return -1000;
}
constexpr int f_wait(int s) { const int a = f_cum(s) - f_hend(s), b = (s >= 1 && s <= 12) ? f_cum(s) - f_wend(s + 1) : 63; return a < b ? a : b; }

#define STAMP(i) do { } while (0)
#define FIN() do { } while (0)
#define DIAGPAR
#define DIAGARG

struct HalfFrag { s16x4 lo, hi; };
DI bf16x8 join(const HalfFrag& h) { return __builtin_shufflevector(h.lo, h.hi, 0, 1, 2, 3, 4, 5, 6, 7); }
DI void rd_tr(HalfFrag& h, unsigned base, unsigned c, unsigned d) {
    if (c + d < 65536) { h.lo = ldstr(base + c); h.hi = ldstr(base + c + d); }
    else { unsigned a = base + c; PINV(a); h.lo = ldstr(a); h.hi = ldstr(a + d); }
}

struct P1 {
    unsigned xa[2], wa[2][2];
    u32x4 rx, rq, r1, rw;
    unsigned xv0, xv1, v0, v1, v2;
    unsigned m1, wk, c0;
    rsrc_t rs; unsigned gb, gs, tag, lane;
    u32x4 srow; float sv[8], sm, sq;
};
template <int N> DI void piece(const P1& P) {
    if constexpr (N < 28) {
        const unsigned kc = wrap7(P.c0 + (N >> 2)) * 128;
        if constexpr ((N & 3) == 0) dma16<phys(N)>(P.xv0, P.rx, kc, P.wk);
        else if constexpr ((N & 3) == 1) dma16<phys(N)>(P.xv1, P.rx, kc, P.wk);
        else if constexpr ((N & 3) == 2) dma16<phys(N)>(P.v0, P.rq, kc * 1344, P.wk);
        else dma16<phys(N)>(P.v1, P.r1, kc * P.m1, P.wk);
    } else if constexpr (N < 35) {
        dma16<phys(N)>(P.v2, P.rw, wrap7(P.c0 + (N - 28)) * (128 * 1792), P.wk);
    }
}
template <int S> DI void rd_ax(bf16x8 (&xf)[14][2], int tt, const P1& P) { if constexpr (S < 14) xf[S][tt] = lds128(P.xa[S & 1] + phys(4 * (S >> 1)) + tt * 2048); }
template <int S> DI void rd_aw(HalfFrag (&wf)[2][2][2], int b, int nt, const P1& P) { if constexpr (S < 14) rd_tr(wf[S & 1][b][nt], P.wa[S & 1][nt], phys(4 * (S >> 1) + 2 + b), 512); }
constexpr int a_first(int s) { return s == 0 ? 8 : (s == 1 ? 12 : (s == 2 ? 16 : 14 + 2 * s)); }
constexpr int a_count(int s) { return s <= 2 ? 4 : 2; }
DI u32x2 ld_gran(const P1& P) { unsigned ln = threadIdx.x; PINV(ln); return __builtin_amdgcn_raw_buffer_load_b64(P.rs, P.gb + (ln & 31) * 8, 0, 16); }
template <int S> DI void stats_stage(P1& P) {
    if constexpr (S == 0) {
#pragma unroll
        for (int j = 0; j < 4; ++j) { const unsigned u = P.srow[j]; P.sv[2 * j] = bflo(u); P.sv[2 * j + 1] = bfhi(u); }
        P.sm = ((P.sv[0] + P.sv[1]) + (P.sv[2] + P.sv[3])) + ((P.sv[4] + P.sv[5]) + (P.sv[6] + P.sv[7])); PINV(P.sm);
    } else if constexpr (S == 1) { P.sm = wavesum(P.sm) * (1.f / 448.f); PINV(P.sm);
    } else if constexpr (S == 2) {
        float q0 = 0.f, q1 = 0.f;
#pragma unroll
        for (int j = 0; j < 4; ++j) { const float d0 = P.sv[j] - P.sm, d1 = P.sv[j + 4] - P.sm; q0 = __builtin_fmaf(d0, d0, q0); q1 = __builtin_fmaf(d1, d1, q1); }
        P.sq = P.lane < 56 ? q0 + q1 : 0.f; PINV(P.sq);
    } else if constexpr (S == 3) { P.sq = wavesum(P.sq) * (1.f / 448.f); PINV(P.sq);
    } else if constexpr (S == 4) {
        const unsigned sdm = pk2(__builtin_amdgcn_sqrtf(rbf(rbf(P.sq) + 1e-5f)), P.sm);
        put_flag(P.rs, P.gs + (P.lane == 0 ? 0 : 0x80000000u), (u32x2){sdm, P.tag});
    }
}
struct Epi { float mr[2], rstd[2], csq[2][2]; f32x4 mq[2], rq[2]; unsigned csw; int s1; };
DI void gran_check(u32x2& R, const P1& P) {
#pragma nounroll
    for (unsigned it = 0; it < SPIN_MAX; ++it) {
        if (__builtin_amdgcn_ballot_w64(R[1] != P.tag) == 0) break;
        __builtin_amdgcn_s_sleep(1);
        asm volatile("" ::: "memory");
        R = ld_gran(P);
    }
}
template <bool B1Q, int S>
DI void step_a(bf16x8 (&xf)[14][2], HalfFrag (&wf)[2][2][2], f32x4 (&acc)[2][2][2], f32x4 (&cs)[2], bf16x8 ones, P1& P, u32x2& R, Epi& E DIAGPAR) {
    if constexpr (S & 1) {
        constexpr int c = (S + 1) >> 1;
        if constexpr (c == 1) WAITBAR(5); else if constexpr (c == 2) WAITBAR(8); else if constexpr (c < 6) WAITBAR(9); else if constexpr (c == 6) WAITBAR(8); else WAITBAR(0);
        STAMP(16 + c);
    }
    constexpr int f = a_first(S), n = a_count(S);
    float ca[2][2], cb_[2][2];
    if constexpr (S == 12) { cs[0] = MFMA(ones, join(wf[0][0][0]), cs[0]); cs[1] = MFMA(ones, join(wf[0][1][0]), cs[1]); SB(); }
    if constexpr (S == 13) {
        const unsigned st = L_ST + (P.wk >> 2);
        unsigned ln = threadIdx.x; PINV(ln);
#pragma unroll
        for (int tt = 0; tt < 2; ++tt) {
            E.rq[tt] = ldsf4(st + tt * 64 + (ln & 48)); E.mq[tt] = ldsf4(st + 128 + tt * 64 + (ln & 48));
            E.rstd[tt] = ldsf(st + tt * 64 + (ln & 15) * 4); E.mr[tt] = ldsf(st + 128 + tt * 64 + (ln & 15) * 4);
        }
#pragma unroll
        for (int i = 0; i < 4; ++i) { const unsigned a = (E.csw & ~(256u | 64u)) + (i >> 1) * 512 + ((i & 1) ^ E.s1) * 64; ca[i >> 1][i & 1] = ldsf(a); cb_[i >> 1][i & 1] = ldsf(a + 256); }
        SB();
    }
#define MA(b, nt, tt) do { acc[b][nt][tt] = (b == 0 || B1Q) ? MFMA(xf[S][tt], join(wf[S & 1][b][nt]), acc[b][nt][tt]) : MFMA(join(wf[S & 1][b][nt]), xf[S][tt], acc[b][nt][tt]); } while (0)
    MA(0, 0, 0); rd_ax<S + 1>(xf, 0, P); SB();
    MA(0, 0, 1); rd_aw<S + 1>(wf, 0, 0, P); SB();
    MA(0, 1, 0); piece<f>(P); SB();
    MA(0, 1, 1); rd_ax<S + 1>(xf, 1, P); SB();
    if constexpr (n > 2) { piece<f + 2>(P); SB(); }
    MA(1, 0, 0); rd_aw<S + 1>(wf, 0, 1, P); SB();
    MA(1, 0, 1); piece<f + 1>(P); SB();
    if constexpr (S == 12) {
        unsigned t1 = threadIdx.x; PINV(t1);
        E.csw += (t1 & 15) * 4;
        *(LAS float*)(size_t)(E.csw) = cs[0][0]; *(LAS float*)(size_t)(E.csw + 512) = cs[1][0]; SB();
        gran_check(R, P);
        const float rs_ = __builtin_amdgcn_rcpf(bflo(R[0])); const unsigned st = L_ST + (P.wk >> 2) + (t1 & 31) * 4;
        *(LAS float*)(size_t)(st) = rs_; *(LAS float*)(size_t)(st + 128) = bfhi(R[0]) * rs_; SB();
    }
    if constexpr (S == 13) {
#pragma unroll
        for (int i = 0; i < 4; ++i) { E.csq[i >> 1][i & 1] = ca[i >> 1][i & 1] + cb_[i >> 1][i & 1]; PINV(E.csq[i >> 1][i & 1]); }
        SB();
    }
    MA(1, 1, 0); rd_aw<S + 1>(wf, 1, 0, P); SB();
    MA(1, 1, 1); rd_aw<S + 1>(wf, 1, 1, P); SB();
#undef MA
    if constexpr ((S & 1) == 0 && S < 12) { cs[0] = MFMA(ones, join(wf[0][0][0]), cs[0]); cs[1] = MFMA(ones, join(wf[0][1][0]), cs[1]); SB(); }
    if constexpr (n > 2) { piece<f + 3>(P); SB(); }
    if constexpr (S <= 4) { stats_stage<S>(P); SB(); }
    if constexpr (S == 10) { R = ld_gran(P); SB(); }
    if constexpr (S < 13) step_a<B1Q, S + 1>(xf, wf, acc, cs, ones, P, R, E DIAGARG);
}
template <bool B1Q>
DI void pass_a(bf16x8 (&xf)[14][2], f32x4 (&acc)[2][2][2], bf16x8 ones, P1& P, Epi& E DIAGPAR) {
    HalfFrag wf[2][2][2]; u32x2 R; f32x4 cs[2] = {ZERO4, ZERO4};
    STAMP(13);
    rd_ax<0>(xf, 0, P); rd_aw<0>(wf, 0, 0, P); rd_ax<0>(xf, 1, P); rd_aw<0>(wf, 0, 1, P); rd_aw<0>(wf, 1, 0, P); rd_aw<0>(wf, 1, 1, P); SB();
    step_a<B1Q, 0>(xf, wf, acc, cs, ones, P, R, E DIAGARG);
}
DI unsigned qkv_dest(unsigned n, unsigned tb, unsigned g) {
    const unsigned hs = (n * 2341u) >> 16, d = n - hs * 28u;
    const unsigned isv = hs >> 5, pl = 448 + 64 * isv;
    unsigned o = WS_Q - isv * (32 * 64 * 256) + (hs * 256 + tb * 4 + g) * pl + d * 16 + ((d >> 3) & 1 & ~isv);
    PINV(o); return o;
}
DI void epi_qkv(const f32x4 (&acc)[2][2], const f32x4 (&mr)[2], const f32x4 (&rq)[2], const float (&cs)[2], const unsigned (&q)[2], rsrc_t rs) {
#pragma unroll
    for (int nt = 0; nt < 2; ++nt) {
        unsigned w[2][2];
#pragma unroll
        for (int tt = 0; tt < 2; ++tt) {
            const f32x4 v = acc[nt][tt] * rq[tt] - mr[tt] * cs[nt];
            w[tt][0] = pk2(v[0], v[1]); w[tt][1] = pk2(v[2], v[3]);
        }
        const bool sw = q[nt] & 1;
        const u32x4 o = {sw ? w[1][0] : w[0][0], sw ? w[1][1] : w[0][1], sw ? w[0][0] : w[1][0], sw ? w[0][1] : w[1][1]};
        __builtin_amdgcn_raw_buffer_store_b128(o, rs, q[nt] & ~15u, 0, 16);
    }
}
DI void epi_ffn(const f32x4 (&acc)[2][2], u32x4 (&o)[2], const unsigned (&ta)[2], int csb, int b1i, bool s1, const float (&mr)[2], const float (&rstd)[2]) {
    f32x4 cs[2], bb[2];
#pragma unroll
    for (int nt = 0; nt < 2; ++nt) {
        cs[nt] = ldsf4(csb + ta[nt] * 2) + ldsf4(csb + 256 + ta[nt] * 2);
        const u32x2 b = *(const LAS u32x2*)(size_t)(L_B1 + b1i * 256 + ta[nt]);
        bb[nt] = (f32x4){bflo(b[0]), bfhi(b[0]), bflo(b[1]), bfhi(b[1])};
    }
#pragma unroll
    for (int tt = 0; tt < 2; ++tt) {
        unsigned w[2][2];
#pragma unroll
        for (int nt = 0; nt < 2; ++nt) {
            f32x4 u = acc[nt][tt] * rstd[tt] + (bb[nt] - mr[tt] * cs[nt]);
            u = __builtin_elementwise_max(u, ZERO4); u = u * u;
            w[nt][0] = pk2(u[0], u[1]); w[nt][1] = pk2(u[2], u[3]);
        }
        o[tt] = s1 ? (u32x4){w[1][0], w[1][1], w[0][0], w[0][1]} : (u32x4){w[0][0], w[0][1], w[1][0], w[1][1]};
    }
}
template <int S> DI void rd_b(HalfFrag (&wf)[3][2], int nt, const unsigned (&wa)[2][2]) { if constexpr (S < 14) rd_tr(wf[S % 3][nt], wa[S & 1][nt], (S >> 1) * UNIT, 512); }
struct PB { u32x4 r, rws; rsrc_t rs; u32x2 ep; unsigned v, wk, f0, f1, c0, qpo, r3, r4; u32x2 pl[1]; };
template <int M> DI void w2_piece(const PB& B) { if constexpr (M < 4) dma16<L_RING + M * UNIT>(B.v, B.r, (5 + 6 * M + 2 * (M == 3 ? (B.r4 & 1) : B.r3)) * (64 * 896), B.wk); }
DI void poll_asm(u32x2& d, unsigned off, u32x4 r) { asm volatile("buffer_load_dwordx2 %0, %1, %2, 0 offen sc1" : "=v"(d) : "v"(off), "s"(r) : "memory"); }
template <int MODE, int S>
DI void step_b(const bf16x8 (&xf)[14][2], HalfFrag (&wf)[3][2], f32x4 (&ac)[2][2], const unsigned (&wa)[2][2], PB& B DIAGPAR) {
    ac[0][0] = MFMA(join(wf[S % 3][0]), xf[S][0], ac[0][0]); rd_b<S + 2>(wf, 0, wa); SB();
    ac[0][1] = MFMA(join(wf[S % 3][0]), xf[S][1], ac[0][1]); SB();
    if constexpr (MODE == 0 && S >= BD0) { w2_piece<S - BD0>(B); SB(); }
    ac[1][0] = MFMA(join(wf[S % 3][1]), xf[S][0], ac[1][0]); rd_b<S + 2>(wf, 1, wa); SB();
    ac[1][1] = MFMA(join(wf[S % 3][1]), xf[S][1], ac[1][1]);
    SB();
    if constexpr (S == HF1_AT) { WAITV(b_upto(MODE, S) + 2); put_flag(B.rs, B.f1 | last_of(L_CNT + 20, 8), B.ep); SB(); }
    if constexpr (S == POLL0) { poll_asm(B.pl[0], B.qpo, B.rws); SB(); }
    if constexpr (S < 13) step_b<MODE, S + 1>(xf, wf, ac, wa, B DIAGARG);
}
template <int MODE>
DI void pass_b(const bf16x8 (&xf)[14][2], f32x4 (&ac)[2][2], HalfFrag (&wf)[3][2], const unsigned (&wa)[2][2], PB& B DIAGPAR) {
#pragma unroll
    for (int i = 0; i < 4; ++i) ac[i >> 1][i & 1] = ZERO4;
    step_b<MODE, 0>(xf, wf, ac, wa, B DIAGARG);
}
DI void pre_b(HalfFrag (&wf)[3][2], unsigned (&wa)[2][2], const P1& P, unsigned base) {
#pragma unroll
    for (int i = 0; i < 4; ++i) { wa[i >> 1][i & 1] = P.wa[i >> 1][i & 1] + base; PINV(wa[i >> 1][i & 1]); }
    rd_b<0>(wf, 0, wa); rd_b<0>(wf, 1, wa); rd_b<1>(wf, 0, wa); rd_b<1>(wf, 1, wa); SB();
}

struct PF { u32x4 rw2, rwo, rws; rsrc_t rs; u32x2 ep, fl, fl2; u64 tbl; unsigned v[4], hv[4], vo, ring, fbo, flo, flo2; const char* xp; u32x4 xres; unsigned wb[4]; };
DI unsigned f_blk(const PF& F, int p) { return (unsigned)(F.tbl >> (8 * p)) & 31u; }
template <int KS> DI void f_ldh(u32x4 (&hf)[14][4], int tt, const PF& F) {
    if constexpr (KS >= 0) { asm volatile("buffer_load_dwordx4 %0, %1, %2, %3 offen offset:%4 sc1" : "=v"(hf[KS][tt]) : "v"(F.hv[tt]), "s"(F.rws), "s"(f_blk(F, KS >> 1) * 2048), "i"((KS & 1) * 1024) : "memory"); SB(); }
}
template <int KS> DI void f_rdw(HalfFrag (&wf)[2][4], int nt, const PF& F) { if constexpr (KS < 14) rd_tr(wf[KS & 1][nt], F.wb[nt], rslot(KS), 2048); }
template <int KS, int I> DI void f_dma(const PF& F) { if constexpr (KS >= 0) { dma16<L_RING + rslot(KS) + I * 1024>(F.v[I], F.rw2, (2 * f_blk(F, KS >> 1) + (KS & 1)) * (32 * 896), F.ring); SB(); } }
template <int S, int I> DI void f_wod(const PF& F) {
    if constexpr (I < f_nw(S)) {
        constexpr int j = 4 * (S - WO0) + I;
        if constexpr (j == 7) lds_wait(L_CNT + 4, 4);
        if constexpr (j < 7) dma16i<L_WO2 + j * 4096, (252 + j * 32) * 896>(F.vo, F.rwo, F.ring >> 3); else dma16i<L_WO + (j - 7) * 4096, (j - 7) * 32 * 896>(F.vo, F.rwo, F.ring >> 3);
        SB();
    }
}
template <int S>
DI void step_f(u32x4 (&hf)[14][4], HalfFrag (&wf)[2][4], f32x4 (&ac)[4][4], PF& F DIAGPAR) {
    STAMP(32 + S);
    if constexpr (S == 2) { asm volatile("s_waitcnt vmcnt(%1)" : "+v"(F.fl) : "i"(f_all(1)) : "memory"); if (!all_set(F.fl, F.ep)) spin_flags(F.rs, F.flo, F.ep); }
    if constexpr (S == 8) { asm volatile("s_waitcnt vmcnt(%1)" : "+v"(F.fl2) : "i"(f_all(7)) : "memory"); if (!all_set(F.fl2, F.ep)) spin_flags(F.rs, F.flo2, F.ep); }
    asm volatile("s_waitcnt vmcnt(%4)" : "+v"(hf[S][0]), "+v"(hf[S][1]), "+v"(hf[S][2]), "+v"(hf[S][3]) : "i"(f_wait(S)) : "memory");
    if constexpr (S == WO0 + WOS) { WAITV(f_cum(S) - (f_cum(WO0 + 4) - f_np(WO0 + 3))); lds_arrive(L_CNT + 8); }
#define MF(tt, nt) do { ac[tt][nt] = MFMA(join(wf[S & 1][nt]), asf(hf[S][tt]), S == 0 ? ZERO4 : ac[tt][nt]); } while (0)
    MF(0, 0); f_dma<fw(S), 0>(F); MF(0, 1); SB(); f_dma<fw(S), 1>(F); MF(0, 2); SB(); f_dma<fw(S), 2>(F); MF(0, 3); SB(); f_dma<fw(S), 3>(F);
    MF(1, 0); f_rdw<S + 1>(wf, 0, F); SB(); f_ldh<fh(S, 0)>(hf, 0, F); MF(1, 1); SB(); f_ldh<fh(S, 0)>(hf, 1, F);
    MF(1, 2); f_rdw<S + 1>(wf, 1, F); SB(); f_ldh<fh(S, 0)>(hf, 2, F); MF(1, 3); SB(); f_ldh<fh(S, 0)>(hf, 3, F);
    MF(2, 0); f_rdw<S + 1>(wf, 2, F); SB(); f_ldh<fh(S, 1)>(hf, 0, F); MF(2, 1); SB(); f_ldh<fh(S, 1)>(hf, 1, F);
    MF(2, 2); f_rdw<S + 1>(wf, 3, F); SB(); f_ldh<fh(S, 1)>(hf, 2, F); MF(2, 3); SB(); f_ldh<fh(S, 1)>(hf, 3, F);
    f_wod<S, 0>(F); MF(3, 0); SB(); f_wod<S, 1>(F); MF(3, 1); SB(); f_wod<S, 2>(F); MF(3, 2); SB(); f_wod<S, 3>(F); MF(3, 3); SB();
#undef MF
    if constexpr (S == 0) { poll_asm(F.fl, F.flo, F.rws); SB(); }
    if constexpr (S == 6) { poll_asm(F.fl2, F.flo2, F.rws); SB(); }
    if constexpr (S == 10) { F.xres = *(const GAS u32x4*)F.xp; SB(); }
    if constexpr (S < 13) step_f<S + 1>(hf, wf, ac, F DIAGARG);
}
template <int S>
DI void step_x(const bf16x8 (&xf)[14][2], HalfFrag (&wf)[2][4], f32x4 (&ac)[4][2], f32x4 (&cs)[4], bf16x8 ones, const unsigned (&wa)[2][4]) {
#pragma unroll
    for (int nt = 0; nt < 4; ++nt) {
        ac[nt][0] = MFMA(join(wf[S & 1][nt]), xf[S][0], ac[nt][0]);
        if constexpr (S < 13) rd_tr(wf[(S + 1) & 1][nt], wa[(S + 1) & 1][nt], ((S + 1) >> 1) * UNIT, 512);
        SB();
        ac[nt][1] = MFMA(join(wf[S & 1][nt]), xf[S][1], ac[nt][1]); cs[nt] = MFMA(join(wf[S & 1][nt]), ones, cs[nt]); SB();
    }
    if constexpr (S < 13) step_x<S + 1>(xf, wf, ac, cs, ones, wa);
}

struct Rest { const char *x, *wo; char *out, *ws; u32x4 rw2; unsigned wv, hd, art, rt64, cb, fb2, s1, hst, fhw, wv3, l0; bool extra, last; };
template <bool SPARE>
DI void rest(const bf16x8 (&xf)[14][2], HalfFrag (&wfb)[3][2], unsigned (&wab)[2][2], u32x4 ones4, const P1& P, PB& B, const Epi& E, const unsigned (&ta)[2], const Rest& A DIAGPAR) {
    const bf16x8 ones = asf(ones4); const rsrc_t rs = B.rs; const u32x2 ep = B.ep; const unsigned wv = A.wv, hd = A.hd, art = A.art, rt64 = A.rt64, w4 = wv & 3;
    f32x4 ac2[2][2];
    pass_b<SPARE ? 1 : 0>(xf, ac2, wfb, wab, B DIAGARG);
    STAMP(3);
    unsigned tid2 = threadIdx.x; PINV(tid2);
    const unsigned lane2 = tid2 & 63, r16b = lane2 & 15, gb = lane2 >> 4, q4b = r16b >> 2, p4b = lane2 & 3;
    const unsigned oxo = (rt64 * 64 + (tid2 >> 3)) * 896 + A.cb * 128 + (tid2 & 7) * 16, fbo = A.fhw + A.fb2 * 128;
    u32x4 ob[2], xres;
    WAITV(1); lds_arrive(L_CNT + 52);
    lds_wait(L_CNT + 48, 8);
    epi_ffn(ac2, ob, ta, L_CS2, 1, A.s1, E.mr, E.rstd);
#pragma unroll
    for (int tt = 0; tt < 2; ++tt) __builtin_amdgcn_raw_buffer_store_b128(ob[tt], rs, A.hst + A.fb2 * 2048 + tt * 56 * 1024, 0, 16);
    STAMP(29);
    if (wv < 4) {
        const u32x4 rQ = mkrsrc(A.ws + WS_Q + hd * (64 * 1792), (4 * art + 4) * 1792), rK = mkrsrc(A.ws + WS_K + hd * (64 * 1792), (4 * art + 4) * 1792), rV = mkrsrc(A.ws + WS_V + hd * (64 * 2048), 64 * 2048);
        unsigned kv0 = tid2 * 16 + (4 * art - 8) * 1792, vv0 = tid2 * 16 + (4 * art - 8) * 2048;
        PINV(kv0); PINV(vv0);
        *(LAS u32x4*)(size_t)(L_V + min(lane2, 11u) * 2048 + 28 * 16) = ones4;
        asm volatile("s_waitcnt vmcnt(2)" : "+v"(B.pl[0])::"memory");
        if (wv == 0) { if (!all_set(B.pl[0], ep)) spin_flags(rs, B.qpo, ep); lds_arrive(L_CNT + 36); } else lds_wait(L_CNT + 36, 1);
        STAMP(28);
        dma16c<L_Q>(kv0 + 8 * 1792, rQ, P.wk); dma16c<L_Q + 4096>(kv0 + 8 * 1792 + 4096, rQ, P.wk);
        dma16c<L_K>(kv0, rK, P.wk); dma16c<L_K + 4096>(kv0 + 4096, rK, P.wk); dma16c<L_K + 2 * 4096>(kv0 + 2 * 4096, rK, P.wk);
        dma16c<L_K + 3 * 4096>(kv0 + 3 * 4096, rK, P.wk); dma16c<L_K + 4 * 4096>(kv0 + 4 * 4096, rK, P.wk); dma16c<L_K + 5 * 4096>(kv0 + 5 * 4096, rK, P.wk);
        dma16v<L_V>(vv0, rV, P.wk); dma16v<L_V + 4096>(vv0 + 4096, rV, P.wk); dma16v<L_V + 2 * 4096>(vv0 + 2 * 4096, rV, P.wk);
        dma16v<L_V + 3 * 4096>(vv0 + 3 * 4096, rV, P.wk); dma16v<L_V + 4 * 4096>(vv0 + 4 * 4096, rV, P.wk); dma16v<L_V + 5 * 4096>(vv0 + 5 * 4096, rV, P.wk);
        __builtin_amdgcn_s_setprio(PRIO_A);
    }
    if (wv >= 4) {
        if constexpr (!SPARE) {
            __builtin_amdgcn_s_setprio(PRIO_F);
            PF F; F.rw2 = A.rw2; F.rwo = mkrsrc(A.wo, 448 * 896); F.rws = B.rws; F.rs = rs; F.ep = ep; F.ring = w4 * UNIT; F.xp = A.x + oxo; F.fbo = fbo;
            F.tbl = ktable(w4, B.r3, B.r4);
#pragma unroll
            for (int i = 0; i < 4; ++i) {
                F.hv[i] = WS_H + (4 * rt64 + i) * (56 * 1024) + lane2 * 16;
                F.v[i] = ((lane2 >> 3) + 8 * i) * 896 + (((lane2 ^ (lane2 >> 3 & 6)) & 7) << 4) + A.cb * 128;
            }
            u32x4 hf[14][4];
            asm volatile("s_waitcnt vmcnt(2)" : "+v"(B.pl[0])::"memory");
            if (!all_set(B.pl[0], ep)) spin_flags(rs, B.qpo, ep);
#pragma unroll
            for (int i = 0; i < 4; ++i) f_ldh<0>(hf, i, F);
#pragma unroll
            for (int i = 0; i < 4; ++i) f_ldh<1>(hf, i, F);
            lds_wait(L_CNT + 52, 8);
            f_dma<2, 0>(F); f_dma<2, 1>(F); f_dma<2, 2>(F); f_dma<2, 3>(F);
            WAITV(12); put_flag(rs, fbo | last_of(L_CNT + 24, 8), ep);
            F.vo = F.v[0] + w4 * 7168;
            const unsigned fhb = WS_FH + (rt64 >> 1) * (28 * 128);
            F.flo = fhb + ((unsigned)(F.tbl >> (8 * min(2 + lane2, 5u))) & 31u) * 128; F.flo2 = fhb + f_blk(F, 6) * 128;
            const unsigned key3 = (2 * gb + (q4b >> 1)) & 3;
#pragma unroll
            for (unsigned nt = 0; nt < 4; ++nt) { F.wb[nt] = L_RING + F.ring + (4 * gb + q4b) * 128 + ((nt ^ key3) << 5) + p4b * 8; PINV(F.wb[nt]); }
            HalfFrag wf[2][4]; f32x4 ac[4][4];
#pragma unroll
            for (int nt = 0; nt < 4; ++nt) f_rdw<0>(wf, nt, F);
            STAMP(5);
            step_f<0>(hf, wf, ac, F DIAGARG);
            STAMP(8);
#pragma unroll
            for (int i = 0; i < 16; ++i) *(LAS f32x4*)(size_t)(L_RING + F.ring + (i >> 3) * 4 * UNIT + ((i >> 2) & 1) * 4096 + r16b * 256 + ((((i & 3) * 4 + gb) ^ r16b) << 4)) = ac[i >> 2][i & 3];
            xres = F.xres;
            BAR();
        } else {
            asm volatile("s_waitcnt vmcnt(0)" : "+v"(B.pl[0])::"memory");
            put_flag(rs, fbo | last_of(L_CNT + 24, 8), ep);
            if (A.extra) {
                lds_wait(L_CNT + 52, 8);
                const unsigned v0 = B.v - 32 * 3584;
#define X27(i) do { const unsigned so = wrap7(B.c0 + i) * (128 * 1792); dma16<L_REG + i * UNIT - 4096>(v0, B.r, so, B.wk); dma16<L_REG + i * UNIT>(B.v, B.r, so, B.wk); } while (0)
                X27(0); X27(1); X27(2); X27(3); X27(4); X27(5); X27(6);
#undef X27
                WAITV(0);
                lds_arrive(L_CNT + 40); lds_wait(L_CNT + 40, 4);
                unsigned wa[2][4];
#pragma unroll
                for (int i = 0; i < 8; ++i) { wa[i >> 2][i & 3] = (P.wa[i >> 2][0] ^ (((2 + A.s1) ^ (i & 3)) << 5)) + L_REG; PINV(wa[i >> 2][i & 3]); }
                HalfFrag wf[2][4]; f32x4 ac[4][2], cs[4];
#pragma unroll
                for (int nt = 0; nt < 4; ++nt) { ac[nt][0] = ZERO4; ac[nt][1] = ZERO4; cs[nt] = ZERO4; rd_tr(wf[0][nt], wa[0][nt], 0, 512); }
                step_x<0>(xf, wf, ac, cs, ones, wa);
#pragma unroll
                for (int c = 0; c < 2; ++c) {
                    f32x4 bb[2];
#pragma unroll
                    for (int n = 0; n < 2; ++n) { const u32x2 b = *(const LAS u32x2*)(size_t)(L_B1 + c * 64 + n * 32 + gb * 8); bb[n] = (f32x4){bflo(b[0]), bfhi(b[0]), bflo(b[1]), bfhi(b[1])}; }
#pragma unroll
                    for (int tt = 0; tt < 2; ++tt) {
                        f32x4 u0 = ac[2 * c][tt] * E.rstd[tt] + (bb[0] - E.mr[tt] * cs[2 * c]), u1 = ac[2 * c + 1][tt] * E.rstd[tt] + (bb[1] - E.mr[tt] * cs[2 * c + 1]);
                        u0 = __builtin_elementwise_max(u0, ZERO4); u0 = u0 * u0; u1 = __builtin_elementwise_max(u1, ZERO4); u1 = u1 * u1;
                        __builtin_amdgcn_raw_buffer_store_b128((u32x4){pk2(u0[0], u0[1]), pk2(u0[2], u0[3]), pk2(u1[0], u1[1]), pk2(u1[2], u1[3])}, rs, A.hst + 27 * 2048 + (c - 1) * 1024 + tt * 56 * 1024, 0, 16);
                    }
                }
                WAITV(0);
                put_flag(rs, (A.fhw + 27 * 128) | last_of(L_CNT + 28, 4), ep);
            }
            FIN();
            __builtin_amdgcn_endpgm();
        }
    } else {
        WAITV(14); put_flag(rs, fbo | last_of(L_CNT + 24, 8), ep);
        unsigned ka0 = L_K + wv * 1792 + p4b * 448 + (8 * gb + q4b) * 16 + ((gb & 1) << 3), ka1 = ka0 ^ 8;
        unsigned va0 = L_V + wv * 2048 + gb * 512 + r16b * 16, va1 = L_V + wv * 2048 + vsel(p4b == 3, 28 * 16, gb * 512 + (16 + 3 * q4b + p4b) * 16);
        unsigned ost = WS_O + ((8 * art + 2 * wv) * 16 + hd) * 1024 + lane2 * 16;
        f32x4 cml, cmh;
#pragma unroll
        for (int j = 0; j < 4; ++j) { const bool newer = 4 * gb + j > r16b; cml[j] = newer ? 0.f : -INFINITY; cmh[j] = newer ? -INFINITY : 0.f; }
        PINV(ka0); PINV(ka1); PINV(va0); PINV(va1); PINV(ost); PINV(cml); PINV(cmh);
        WAITV(7); lds_arrive(L_CNT); lds_wait(L_CNT, 4);
        STAMP(5);
        {
            HalfFrag qh[2], kf[18];
            rd_tr(qh[0], ka0 - (L_K - L_Q), 0, 64); rd_tr(qh[1], ka1 - (L_K - L_Q), 0, 64);
            f32x4 s[2][18];
            bf16x8 vf[9][2];
            float m[2] = {-INFINITY, -INFINITY};
#define MX(u, t) do { m[u] = __builtin_fmaxf(__builtin_fmaxf(m[u], s[u][t][0]), s[u][t][1]); m[u] = __builtin_fmaxf(__builtin_fmaxf(m[u], s[u][t][2]), s[u][t][3]); PINV(m[u]); } while (0)
            if (art >= 2) {
#pragma unroll
                for (int t = 0; t < 3; ++t) rd_tr(kf[t], (t & 1) ? ka1 : ka0, (t >> 1) * 1792, 64);
                if (gb == 3) { qh[0].hi = (s16x4){0, 0, 0, 0}; qh[1].hi = (s16x4){0, 0, 0, 0}; }
                const bf16x8 qf0 = join(qh[0]), qf1 = join(qh[1]);
#pragma unroll
                for (int t = 0; t < 18; ++t) {
                    if (t < 17) s[0][t] = MFMA(join(kf[t]), qf0, t == 0 ? cml : (t == 16 ? cmh : ZERO4));
                    if (t + 3 < 18) rd_tr(kf[t + 3], (t & 1) ? ka0 : ka1, ((t + 3) >> 1) * 1792, 64);
                    if (t >= 2) MX(0, t - 2);
                    SB();
                    if (t > 0) s[1][t] = MFMA(join(kf[t]), qf1, t == 1 ? cml : (t == 17 ? cmh : ZERO4));
                    if (t >= 3) MX(1, t - 2);
                    if (t == 12) { WAITV(1); lds_arrive(L_CNT + 44); }
                    SB();
                }
                MX(0, 16); MX(1, 16); MX(1, 17);
            } else {
                const int b0 = 8 - 4 * (int)art - (int)wv;
                WAITV(1); lds_arrive(L_CNT + 44);
                if (gb == 3) { qh[0].hi = (s16x4){0, 0, 0, 0}; qh[1].hi = (s16x4){0, 0, 0, 0}; }
                const bf16x8 qf0 = join(qh[0]), qf1 = join(qh[1]);
                const f32x4 ninf = {-INFINITY, -INFINITY, -INFINITY, -INFINITY};
#pragma unroll
                for (int b = 0; b < 9; ++b) {
                    if (b >= b0) {
                        rd_tr(kf[0], ka0, b * 1792, 64); rd_tr(kf[1], ka1, b * 1792, 64);
                        s[0][2 * b] = MFMA(join(kf[0]), qf0, b == 8 ? cmh : ZERO4); s[1][2 * b] = MFMA(join(kf[0]), qf1, ZERO4);
                        s[0][2 * b + 1] = MFMA(join(kf[1]), qf0, ZERO4); s[1][2 * b + 1] = MFMA(join(kf[1]), qf1, b == 8 ? cmh : ZERO4);
                        MX(0, 2 * b); MX(1, 2 * b); if (b < 8) MX(0, 2 * b + 1); MX(1, 2 * b + 1);
                    } else { s[0][2 * b] = ninf; s[1][2 * b] = ninf; s[0][2 * b + 1] = ninf; s[1][2 * b + 1] = ninf; }
                }
            }
#undef MX
            if constexpr (!SPARE) lds_arrive(L_CNT + 4);
            const float SC = 0.27264391f;
            const float mc[2] = {-SC * redmax4(m[0]), -SC * redmax4(m[1])};
            lds_wait(L_CNT + 44, 4);
#pragma unroll
            for (int b = 0; b < 2; ++b) { vf[b][0] = lds128(va0 + b * 2048); vf[b][1] = lds128(va1 + b * 2048); }
            STAMP(6);
            f32x4 o[2][2] = {{ZERO4, ZERO4}, {ZERO4, ZERO4}};
            u32x4 pw[2];
#pragma unroll
            for (int i = 0; i < 19; ++i) {
                const int b = i >> 1, u = i & 1, pb = (i - 1) >> 1, pu = (i - 1) & 1;
                const bool lo = i < 18 && (u == 0 || b > 0), hi = i < 18 && (u == 1 || b < 8);
                f32x4 ea = ZERO4, eb = ZERO4;
                if (lo) ea = s[u][2 * b] * SC + mc[u];
                if (hi) eb = s[u][2 * b + 1] * SC + mc[u];
                if (u == 0 && b + 2 < 9) { vf[b + 2][0] = lds128(va0 + (b + 2) * 2048); vf[b + 2][1] = lds128(va1 + (b + 2) * 2048); }
                SB();
                if (lo) { ea[0] = __builtin_amdgcn_exp2f(ea[0]); ea[1] = __builtin_amdgcn_exp2f(ea[1]); PINV(ea); }
                SB();
                if (i > 0) o[pu][0] = MFMA(vf[pb][0], asf(pw[pu]), o[pu][0]);
                if (lo) { ea[2] = __builtin_amdgcn_exp2f(ea[2]); ea[3] = __builtin_amdgcn_exp2f(ea[3]); PINV(ea); }
                if (hi) { eb[0] = __builtin_amdgcn_exp2f(eb[0]); eb[1] = __builtin_amdgcn_exp2f(eb[1]); PINV(eb); }
                SB();
                if (i > 0) o[pu][1] = MFMA(vf[pb][1], asf(pw[pu]), o[pu][1]);
                if (hi) { eb[2] = __builtin_amdgcn_exp2f(eb[2]); eb[3] = __builtin_amdgcn_exp2f(eb[3]); PINV(eb); }
                SB();
                if (i < 18) { pw[u] = (u32x4){lo ? pk2(ea[0], ea[1]) : 0u, lo ? pk2(ea[2], ea[3]) : 0u, hi ? pk2(eb[0], eb[1]) : 0u, hi ? pk2(eb[2], eb[3]) : 0u}; PINV(pw[u]); }
                SB();
            }
#pragma unroll
            for (int u = 0; u < 2; ++u) {
                const float il = __builtin_amdgcn_rcpf(o[u][1][3]);
                __builtin_amdgcn_raw_buffer_store_b128((u32x4){pk2(o[u][0][0] * il, o[u][0][1] * il), pk2(o[u][0][2] * il, o[u][0][3] * il), pk2(o[u][1][0] * il, o[u][1][1] * il), pk2(o[u][1][2] * il, 0.f)}, rs, ost + u * 16384, 0, 16);
            }
        }
        STAMP(7);
        const unsigned fo = WS_FO + (art * 16 + hd) * 128 + A.l0;
        WAITV(0);
        put_flag(rs, fo | last_of(L_CNT + 32, 4), ep);
        __builtin_amdgcn_s_setprio(0);
        if constexpr (SPARE) {
            if (A.last && wv == 0) {
                __builtin_amdgcn_s_sleep(64);
#pragma nounroll
                for (unsigned it = 0; it < SPIN_MAX; ++it) {
                    unsigned bad = 0;
                    asm volatile("" ::: "memory");
#pragma unroll
                    for (int i = 0; i < 4; ++i) { const u32x2 f = get_flag(rs, WS_FO + (i * 64 + lane2) * 128); bad |= (f[0] ^ ep[0]) | (f[1] ^ ep[1]); }
                    if (__builtin_amdgcn_ballot_w64(bad != 0) == 0) break;
                    __builtin_amdgcn_s_sleep(64);
                }
                put_flag(rs, WS_E + A.l0, ep);
            }
            asm volatile(".Lfk_end:");
            FIN();
            __builtin_amdgcn_endpgm();
        } else {
            xres = *(const GAS u32x4*)(A.x + oxo);
            const unsigned d0 = 4 * gb + q4b, d1 = 16 + 3 * gb + min(q4b, 2u);
            unsigned a0[4], a1[4], a2[4], a3[4];
#pragma unroll
            for (unsigned nt = 0; nt < 4; ++nt) {
                a0[nt] = L_WO + d0 * 128 + ((nt ^ ((d0 >> 1) & 3)) << 5) + p4b * 8; a1[nt] = L_WO + d1 * 128 + ((nt ^ ((d1 >> 1) & 3)) << 5) + p4b * 8;
                a2[nt] = a0[nt] + L_WO2; a3[nt] = a1[nt] + L_WO2;
                PINV(a0[nt]); PINV(a1[nt]); PINV(a2[nt]); PINV(a3[nt]);
            }
            const unsigned foff = WS_FO + ((rt64 >> 1) * 16 + r16b) * 128, obase = WS_O + (4 * rt64 + wv) * 16384 + lane2 * 16;
            HalfFrag wof[16][4];
#define RWO(i) do { constexpr int i_ = (i) < 9 ? (i) : (i) - 9, o_ = i_ * 3584; _Pragma("unroll") for (int nt = 0; nt < 4; ++nt) { \
                wof[i][nt].lo = ldstr(((i) < 9 ? a0 : a2)[nt ^ ((i_ & 1) << 1)] + o_); wof[i][nt].hi = ldstr(((i) < 9 ? a1 : a3)[nt ^ ((i_ & 1) << 1)] + o_); } } while (0)
            lds_wait(L_CNT + 8, 4);
            STAMP(30);
            RWO(0); RWO(1); RWO(2); RWO(3); RWO(4); RWO(5);
            SB();
            if (wv == 0) { spin_flags(rs, foff, ep); lds_arrive(L_CNT + 12); } else lds_wait(L_CNT + 12, 1);
            STAMP(9);
            u32x4 of[16];
#pragma unroll
            for (int i = 0; i < 16; ++i) of[i] = __builtin_amdgcn_raw_buffer_load_b128(rs, obase + (i & 3) * 1024, (i >> 2) * 4096, 16);
            SB();
            f32x4 ac[4] = {ZERO4, ZERO4, ZERO4, ZERO4};
#define MWO(i) do { _Pragma("unroll") for (int nt = 0; nt < 4; ++nt) ac[nt] = MFMA(join(wof[i][nt]), asf(of[i]), ac[nt]); } while (0)
            MWO(0); RWO(6); SB();
            MWO(1); RWO(7); SB();
            MWO(2); RWO(8); SB();
            MWO(3); RWO(9); SB();
            MWO(4); RWO(10); SB();
            MWO(5); RWO(11); SB();
            MWO(6); RWO(12); SB();
            MWO(7); RWO(13); SB();
            MWO(8); RWO(14); SB();
            MWO(9); RWO(15); SB();
            MWO(10); SB();
            MWO(11); SB();
            MWO(12); SB();
            MWO(13); SB();
            MWO(14); SB();
            MWO(15); SB();
#undef MWO
#undef RWO
            STAMP(8);
            BAR();
#pragma unroll
            for (int nt = 0; nt < 4; ++nt) *(LAS f32x4*)(size_t)(L_RING + (8 + wv) * UNIT + r16b * 256 + (((nt * 4 + gb) ^ r16b) << 4)) = ac[nt];
        }
    }
    if constexpr (!SPARE) {
        BAR();
        STAMP(10);
        const unsigned row = tid2 >> 3, rr = (row & 15) * 256 + ((((tid2 & 7) * 2) ^ (row & 15)) << 4);
        const unsigned ra = L_RING + (row >> 5) * 4 * UNIT + ((row >> 4) & 1) * 4096 + rr, r5 = L_RING + (8 + (row >> 4)) * UNIT + rr;
        f32x4 s0 = ldsf4(r5), s1 = ldsf4(r5 ^ 16);
#pragma unroll
        for (int f = 0; f < 4; ++f) { s0 += ldsf4(ra + f * UNIT); s1 += ldsf4((ra ^ 16) + f * UNIT); }
        const u32x4 bb = __builtin_bit_cast(u32x4, lds128(L_B2 + (tid2 & 7) * 16));
        u32x4 res;
#pragma unroll
        for (int j = 0; j < 4; ++j) {
            const unsigned xj = xres[j], bj = bb[j];
            const float u0 = j < 2 ? s0[2 * j] : s1[2 * j - 4], u1 = j < 2 ? s0[2 * j + 1] : s1[2 * j - 3];
            res[j] = pk2(u0 + bflo(xj) + bflo(bj), u1 + bfhi(xj) + bfhi(bj));
        }
        *(GAS u32x4*)(A.out + oxo) = res;
    }
}

__global__ void __launch_bounds__(512, 2) fused_k(const char* __restrict__ x, const char* __restrict__ wqkv, const char* __restrict__ wo, const char* __restrict__ w1,
                                                  const char* __restrict__ b1, const char* __restrict__ w2, const char* __restrict__ b2, char* __restrict__ out, char* ws) {
    STAMP(0);
    const unsigned tid = threadIdx.x, lane = tid & 63, wv = __builtin_amdgcn_readfirstlane(tid >> 6);
    const unsigned bid = blockIdx.x, xcd = bid & 7, idx = bid >> 3;
    {
        u64 pc; unsigned len;
        asm volatile("s_getpc_b64 %0\n.Lfk_begin:\n\ts_mov_b32 %1, .Lfk_end-.Lfk_begin-256" : "=s"(pc), "=s"(len));
        gdma4(lane * 4, (const char*)(pc + min(idx * 1536 + wv * 256, len)), L_JUNK + wv * 256);
    }
    const unsigned rt = (xcd & 1) * 8 + (idx & 7), G = xcd >> 1, slot = G * 4 + (idx >> 3);
    const bool two = slot < 5;
    const unsigned qb0 = two ? 2 * slot : slot + 5;
    const unsigned blk1 = two ? 2 * slot + 1 : 2 * slot - 5;
    const unsigned fb2 = two ? slot : 2 * slot - 4;
    const unsigned drow = tid >> 3, dc = lane & 7;
    P1 P;
    P.wk = wv * 1024; P.c0 = (rt + slot) - 7 * (((rt + slot) * 37) >> 8); P.lane = lane;
    P.xv0 = (rt * 128 + drow) * 896 + ((dc ^ ((drow >> 1) & 7)) << 4); P.xv1 = P.xv0 + 64 * 896;
    const unsigned wsw1 = (dc ^ ((drow & 2) | ((drow >> 1) & 4))) << 4;
    P.v0 = drow * 2688 + wsw1 + qb0 * 128; P.v2 = drow * 3584 + wsw1;
    P.m1 = two ? 1344 : 1792; P.v1 = drow * 2 * P.m1 + wsw1 + blk1 * 128;
    const unsigned srow_off = (rt * 128 + slot * 8 + wv) * 896 + lane * 16 + (lane < 56 ? 0 : OOB);
    asm volatile("" : "+s"(x), "+s"(wqkv), "+s"(wo), "+s"(w1), "+s"(b1), "+s"(w2), "+s"(b2), "+s"(out), "+s"(ws));
    P.rx = mkrsrc(x, 2048 * 896); P.rq = mkrsrc(wqkv, 448 * 2688); P.rw = mkrsrc(w1, 448 * 3584); P.r1 = two ? P.rq : P.rw;
    const u64 e0 = *(const __attribute__((address_space(4))) u64*)(ws + WS_E);
    asm volatile("buffer_load_dwordx4 %0, %1, %2, 0 offen" : "=v"(P.srow) : "v"(srow_off), "s"(P.rx) : "memory");
    piece<0>(P); piece<1>(P); piece<2>(P); piece<3>(P);
    const bool spare = idx - (G == 0 ? 24u : 28u) < 4u;
    const bool extra = spare && G < 2;
    const unsigned p3 = spare ? 0 : (idx >= 28 ? idx - 4 : idx);
    const unsigned p37 = (p3 * 37) >> 8, rt64 = 4 * xcd + p37, cb = p3 - 7 * p37;
    {
        const unsigned w3 = wv & 3, sel = 27u | (fb2 << 8) | ((blk1 & 31) << 16) | (cb << 24);
        gdma4((lane & 31) * 4 + ((sel >> (8 * w3)) & 31) * 128, w3 == 3 ? b2 : b1, L_B1 + w3 * 256 + (wv >> 2) * 4096);
    }
    piece<4>(P); piece<5>(P); piece<6>(P); piece<7>(P);
    {
        const unsigned k = bid * 8 + wv, k3 = min(k, 2015u) - 1960;
        gdma4(k < 1960 ? k * 1024 + (lane & 7) * 128 : (k3 * 8 + (lane & 7)) * 3584, k < 1568 ? w2 : (k < 1960 ? wo - 1568 * 1024 : w1 + 27 * 128), L_JUNK + wv * 256);
    }
    P.v2 += fb2 * 128;
    STAMP(12);
    const unsigned r16 = lane & 15, g = lane >> 4, q4 = r16 >> 2, p4 = lane & 3;
    const unsigned slab = wv & 3, ch = wv >> 2, s1 = slab >> 1;
    const u64 ep64 = e0 * 0xD1342543DE82EF95ull + 0x9E3779B97F4A7C15ull;
    const u32x2 ep = {(unsigned)ep64, (unsigned)(ep64 >> 32)};
    const rsrc_t rs = __builtin_amdgcn_make_buffer_rsrc(ws, 0, (int)WS_END, 0x00020000);
    {
        const unsigned fl = r16 >> 1, key = ((q4 >> 1) & 1) | ((g & 1) << 1);
#pragma unroll
        for (unsigned e = 0; e < 2; ++e) {
            const unsigned ee = e ^ ch ^ (slab & 1);
            P.xa[e] = (slab >> 1) * UNIT + ((slab & 1) * 32 + r16) * 128 + (((4 * ee + g) ^ fl) << 4);
#pragma unroll
            for (unsigned nt = 0; nt < 2; ++nt) P.wa[e][nt] = ee * 4096 + (8 * g + q4) * 128 + (((2 * ch + (nt ^ s1)) ^ key) << 5) + p4 * 8;
        }
    }
    const unsigned tb = 4 * rt + slab;
    P.rs = rs; P.tag = ep[1]; P.gb = WS_GS + tb * 256; P.gs = WS_GS + (rt * 128 + slot * 8 + wv) * 8;
    unsigned qd[2][2];
#pragma unroll
    for (unsigned nt = 0; nt < 2; ++nt) {
        const unsigned nl = 32 * ch + 16 * (nt ^ s1);
        qd[0][nt] = qkv_dest(64 * qb0 + nl + r16, tb, g); qd[1][nt] = qkv_dest(64 * (two ? blk1 : qb0) + nl + r16, tb, g);
    }
    u32x4 ones4 = {0x3f803f80u, 0x3f803f80u, 0x3f803f80u, 0x3f803f80u}; PINV(ones4);
    const bf16x8 ones = asf(ones4);
    *(LAS u32x4*)(size_t)(L_CNT + (lane & 3) * 16) = (u32x4){0u, 0u, 0u, 0u};
    bf16x8 xf[14][2];
    f32x4 acc[2][2][2];
#pragma unroll
    for (int i = 0; i < 8; ++i) acc[i >> 2][(i >> 1) & 1][i & 1] = ZERO4;
    Epi E; E.csw = L_CS + (slab & 1) * 256 + (32 * ch + 16 * s1) * 4; E.s1 = s1;
    asm volatile("s_waitcnt vmcnt(6) lgkmcnt(0)\n\ts_barrier" : "+v"(P.srow)::"memory");
    if (two) pass_a<true>(xf, acc, ones, P, E DIAGARG);
    else pass_a<false>(xf, acc, ones, P, E DIAGARG);
    STAMP(1);
    __builtin_amdgcn_s_setprio(2);
    epi_qkv(acc[0], E.mq, E.rq, E.csq[0], qd[0], rs);
    if (two) epi_qkv(acc[1], E.mq, E.rq, E.csq[1], qd[1], rs);
    __builtin_amdgcn_s_setprio(0);
    STAMP(2);
    unsigned tid1 = threadIdx.x; PINV(tid1);
    const unsigned hst = WS_H + (2 * tb * 56 + ch) * 1024 + (tid1 & 63) * 16;
    const unsigned ta[2] = {64 * ch + 32 * s1 + ((tid1 & 48) >> 1), 64 * ch + 32 * (s1 ^ 1) + ((tid1 & 48) >> 1)};
    const unsigned wv3 = (tid1 >> 3) * 896 + (((tid1 ^ (tid1 >> 3 & 6)) & 7) << 4) + cb * 128, l0 = (tid1 & 63) == 0 ? 0 : OOB;
    const u32x4 rw2 = mkrsrc(w2, 1792 * 896);
    const unsigned fhw = WS_FH + rt * 28 * 128 + l0;
    PB B; B.rs = rs; B.ep = ep; B.wk = P.wk; B.c0 = P.c0;
    { const unsigned r = ROT ? cb + p37 : 0; B.r4 = r & 3; B.r3 = r - 3 * ((r * 11) >> 5); }
    B.f0 = WS_FQ + (rt * 21 + qb0) * 128 + l0; B.f1 = two ? OOB : fhw + blk1 * 128;
    if (!two) {
        u32x4 od[2]; epi_ffn(acc[1], od, ta, L_CS + 512, 2, s1, E.mr, E.rstd);
#pragma unroll
        for (int tt = 0; tt < 2; ++tt) __builtin_amdgcn_raw_buffer_store_b128(od[tt], rs, hst + blk1 * 2048 + tt * 56 * 1024, 0, 16);
    }
    HalfFrag wfb[3][2]; unsigned wab[2][2];
    {
        unsigned a = P.wa[0][0] + L_REG; PINV(a);
        HalfFrag cf[7]; f32x4 c2 = ZERO4;
#pragma unroll
        for (int i = 0; i < 7; ++i) rd_tr(cf[i], a, i * UNIT, 512);
#pragma unroll
        for (int i = 0; i < 7; ++i) c2 = MFMA(ones, join(cf[i]), c2);
        pre_b(wfb, wab, P, L_REG);
        *(LAS float*)(size_t)(E.csw + (L_CS2 - L_CS)) = c2[0];
    }
    const unsigned hd = 2 * xcd + (idx & 1), art = idx >> 1;
    B.rws = mkrsrc(ws, WS_END);
    {
        const unsigned ln = min(tid1 & 63, 13u), sel = ln >= 7, m = ln - 7 * sel, sec = (m >= 1) + (m >= 4);
        const int t = (int)(art + m) - 3 * (int)sec;
        const unsigned fe = WS_FH + ((rt64 >> 1) * 28 + min(5 + 6 * slab + 2 * min(ln, 2u), 25u)) * 128;
        B.qpo = ch ? fe : WS_FQ + ((t < 0 ? art : t) * 21 + ((448 * sec + 28 * hd + 27 * sel) >> 6)) * 128 + (wv == 0 ? 0 : OOB);
    }
    PINV(B.qpo);
    STAMP(24);
    if (two) WAITV(0); else WAITV(2);
    lds_arrive(L_CNT + 48);
    { const unsigned lo = last_of(L_CNT + 16, 8); put_flag(rs, B.f0 | lo, ep); put_flag(rs, (two ? B.f0 + 128 : OOB) | lo, ep); }
    STAMP(25);
    Rest A; A.x = x; A.wo = wo; A.out = out; A.ws = ws; A.rw2 = rw2; A.wv = wv; A.hd = hd; A.art = art; A.rt64 = rt64; A.cb = cb; A.fb2 = fb2; A.s1 = s1;
    A.hst = hst; A.fhw = fhw; A.wv3 = wv3; A.l0 = l0; A.extra = extra; A.last = bid == 4 + 8 * 28;
    if (spare) {
        B.r = P.rw; B.v = P.v2 + (27 - fb2) * 128; rest<true>(xf, wfb, wab, ones4, P, B, E, ta, A DIAGARG);
        __builtin_unreachable();
    }
    B.r = rw2; B.v = wv3; rest<false>(xf, wfb, wab, ones4, P, B, E, ta, A DIAGARG);
    FIN();
}

extern "C" void kernel_launch(void* const* d_in, const int* in_sizes, int n_in, void* d_out, int out_size, void* d_ws, size_t ws_size, hipStream_t stream) {
    (void)in_sizes; (void)n_in; (void)out_size; (void)ws_size;
    (void)hipFuncSetAttribute((const void*)fused_k, hipFuncAttributeMaxDynamicSharedMemorySize, LDS_BYTES);
    fused_k<<<256, 512, LDS_BYTES, stream>>>((const char*)d_in[0], (const char*)d_in[1], (const char*)d_in[2], (const char*)d_in[4], (const char*)d_in[5],
                                             (const char*)d_in[6], (const char*)d_in[7], (char*)d_out, (char*)d_ws);
}
```
